```python
import torch

def _softmax(x, axis=-1):
    return torch.softmax(x, dim=axis)
def _gelu(x):
    return torch.nn.functional.gelu(x, approximate="tanh")
def _silu(x):
    return torch.nn.functional.silu(x)
def _sigmoid(x):
    return torch.sigmoid(x)
def _layernorm(x, axis=-1, eps=1e-5):
    m = x.mean(dim=axis, keepdim=True)
    v = x.var(dim=axis, keepdim=True, unbiased=False)
    return (x - m) / torch.sqrt(v + eps)
def _scatter_add(tgt, idx, src, axis=0):
    return tgt.clone().index_add_(axis, idx, src)
def _segment_sum(src, seg, n):
    out = torch.zeros((n,) + src.shape[1:], dtype=src.dtype)
    return out.index_add_(0, seg, src)
def _pad(x, pw):
    flat = tuple(v for pair in reversed(pw) for v in pair)
    return torch.nn.functional.pad(x, flat)

kernel_name = 'parallel_sum__softmax_full_mha__mixer_channel_mlp__prompt'

def setup_inputs():

    _w = (lambda a: (lambda t: t.to(torch.bfloat16) if t.is_floating_point() else t)(torch.as_tensor(a)))
    def _A_setup():
        B, S, D = 1, 2048, 640
        return {
            "x": _w((lambda *s: torch.randn(*s, dtype=torch.bfloat16) * 0.05)(B, S, D)),
            "wqkv": _w((lambda *s: torch.randn(*s, dtype=torch.bfloat16) * 0.05)(D, 3 * D)),
            "wo": _w((lambda *s: torch.randn(*s, dtype=torch.bfloat16) * 0.05)(D, D)),
        }
    _A_in = _A_setup()

    def _B_setup():
        B, S, D, F = 1, 2048, 640, 1536
        return {
            "x": _w((lambda *s: torch.randn(*s, dtype=torch.bfloat16) * 0.05)(B, S, D)),
            "w1": _w((lambda *s: torch.randn(*s, dtype=torch.bfloat16) * 3.2)(D, F) * (D ** -0.5)),
            "b1": _w((lambda *s: torch.randn(*s, dtype=torch.bfloat16) * 1.6)(F)),
            "w2": _w((lambda *s: torch.randn(*s, dtype=torch.bfloat16) * 0.1)(F, D) * (F ** -0.5)),
            "b2": _w((lambda *s: torch.randn(*s, dtype=torch.bfloat16) * 0.2)(D)),
        }
    _B_in = _B_setup()

    return {
        "A_x": _A_in["x"],
        "A_wqkv": _A_in["wqkv"],
        "A_wo": _A_in["wo"],
        "B_x": _B_in["x"],
        "B_w1": _B_in["w1"],
        "B_b1": _B_in["b1"],
        "B_w2": _B_in["w2"],
        "B_b2": _B_in["b2"],
    }

def reference(A_x, A_wqkv, A_wo, B_x, B_w1, B_b1, B_w2, B_b2):
    def _A_ref(x, wqkv, wo):
        B, S, D = x.shape
        H = 32; Dh = D // H
        qkv = (lambda x, *d: x.permute(*d))((x @ wqkv).reshape(B, S, 3, H, Dh), 2, 0, 3, 1, 4)
        q, k, v = qkv[0], qkv[1], qkv[2]
        scale = Dh ** -0.5
        scores = torch.einsum("bhqd,bhkd->bhqk", q, k) * scale
        p = _softmax(scores, axis=-1)
        o = torch.einsum("bhqk,bhkd->bhqd", p, v)
        return (lambda x, *d: x.permute(*d))(o, 0, 2, 1, 3).reshape(B, S, D) @ wo

    def _B_ref(x, w1, b1, w2, b2):
        y = _layernorm(x, axis=-1)
        h = _gelu(torch.einsum("bsd,df->bsf", y, w1) + b1)
        return torch.einsum("bsf,fd->bsd", h, w2) + b2

    x = A_x
    xn = _layernorm(x, axis=-1)
    return x + _A_ref(xn, A_wqkv, A_wo) + _B_ref(xn, B_w1, B_b1, B_w2, B_b2)
```

```cpp
#include <hip/hip_runtime.h>
#include <hip/hip_bf16.h>
#include <stdint.h>

typedef __bf16 bf16x8 __attribute__((ext_vector_type(8)));
typedef __bf16 bf16x4 __attribute__((ext_vector_type(4)));
typedef __bf16 bf16x2 __attribute__((ext_vector_type(2)));
typedef float f32x16 __attribute__((ext_vector_type(16)));
typedef float f32x8 __attribute__((ext_vector_type(8)));
typedef float f32x4 __attribute__((ext_vector_type(4)));
typedef float f32x2 __attribute__((ext_vector_type(2)));
typedef short s16x4 __attribute__((ext_vector_type(4)));
typedef unsigned u32x4 __attribute__((ext_vector_type(4)));
typedef unsigned u32x2 __attribute__((ext_vector_type(2)));
#define LAS __attribute__((address_space(3)))
#define DI __device__ __forceinline__
typedef LAS char* lptr;

constexpr int S_ = 2048, D_ = 640, H_ = 32, F_ = 1536;
constexpr int QT = 1280;
constexpr int KVT = 2624;
constexpr int NKS = 136;
constexpr size_t WS_Q = 0;
constexpr size_t WS_KV = WS_Q + (size_t)H_ * 64 * QT;
constexpr size_t WS_AF = WS_KV + (size_t)H_ * 64 * KVT;
constexpr float QSCALE = 0.22360679774997896f * 1.4426950408889634f;

#define MFMA32(a, b, c) __builtin_amdgcn_mfma_f32_32x32x16_bf16((a), (b), (c), 0, 0, 0)
#define MFMA16(a, b, c) __builtin_amdgcn_mfma_f32_16x16x32_bf16((a), (b), (c), 0, 0, 0)
#define STAMP_DECL
#define STAMP(i)
#define STAMP_FLUSH(K)
#define SBAR() __builtin_amdgcn_sched_barrier(0)
#define WAIT_BAR(N) asm volatile("s_waitcnt vmcnt(" #N ") lgkmcnt(0)\n\ts_barrier" ::: "memory")

template <int OFF>
DI void glds16(const void* sbase, unsigned voff, unsigned lds) {
    unsigned keep;
    asm volatile("s_mov_b32 %0, m0\n\ts_mov_b32 m0, %3\n\ts_nop 2\n\tglobal_load_lds_dwordx4 %1, %2 offset:%4\n\ts_mov_b32 m0, %0"
                 : "=&s"(keep) : "v"(voff), "s"(sbase), "s"(lds), "i"(OFF) : "memory");
}
DI bf16x8 lds_b128(lptr p) { return *(const LAS bf16x8*)p; }
DI bf16x4 lds_tr(lptr p) { return __builtin_bit_cast(bf16x4, __builtin_amdgcn_ds_read_tr16_b64_v4i16((LAS s16x4*)p)); }
DI bf16x8 cat8(bf16x4 a, bf16x4 b) { return __builtin_shufflevector(a, b, 0, 1, 2, 3, 4, 5, 6, 7); }
DI float swap32_other(float v) {
    auto r = __builtin_amdgcn_permlane32_swap(__float_as_uint(v), __float_as_uint(v), false, false);
    return (threadIdx.x & 32) ? __uint_as_float(r[0]) : __uint_as_float(r[1]);
}
DI float opq(float v) { asm("" : "+v"(v)); return v; }
DI float dot2(bf16x2 a, bf16x2 b, float c) { return __builtin_amdgcn_fdot2_f32_bf16(a, b, c, false); }
DI float gelu_tanh(float x) {
    const float e = __builtin_amdgcn_exp2f(x * __builtin_fmaf(x * x, -0.044715f * 2.302208198f, -2.302208198f));
    return x * __builtin_amdgcn_rcpf(1.0f + e);
}

template <int MODE, int NST>
DI void gemm1_tile(const char* __restrict__ x, const char* __restrict__ W, int ldw  , int row0, int col0  ,
                   const __bf16* __restrict__ b1, char* __restrict__ ws, lptr lds) {
    STAMP_DECL;
    STAMP(0);
    const int tid = threadIdx.x, l = tid & 63, r = l & 31, hh = l >> 5;
    const int w = __builtin_amdgcn_readfirstlane(tid >> 6), wr = w & 1, wc = w >> 1;
    const unsigned lds0 = (unsigned)(uintptr_t)lds;
    const unsigned va0 = (unsigned)((16 * w + (l >> 3)) * 1280 + (((l & 7) ^ (l >> 4)) << 4));
    const unsigned va1 = (unsigned)((16 * w + 8 + (l >> 3)) * 1280 + (((l & 7) ^ (4 + (l >> 4))) << 4));
    const unsigned vb0 = (unsigned)((8 * w + (l >> 4)) * ldw * 2 + (((l & 15) ^ ((l >> 4) << 2)) << 4));
    const unsigned vb1 = vb0 + (unsigned)(4 * ldw * 2);
    const char* xa = x + (size_t)row0 * 1280;
    const char* wb = W + (size_t)col0 * 2;
    auto stage = [&](int kt) {
        const unsigned dst = lds0 + (unsigned)(kt % NST) * 32768u + (unsigned)w * 2048u;
        const char* pa = xa + kt * 128;
        const char* pb = wb + (size_t)kt * 64 * ldw * 2;
        glds16<0>(pa, va0, dst);
        glds16<0>(pa, va1, dst + 1024);
        glds16<0>(pb, vb0, dst + 16384);
        glds16<0>(pb, vb1, dst + 16384 + 1024);
    };
#pragma unroll
    for (int i = 0; i < NST - 1; ++i) stage(i);
    unsigned aoff[4], boff[4];
    const int g4 = l >> 4, q4 = (l & 15) >> 2, p4 = l & 3;
#pragma unroll
    for (int i = 0; i < 4; ++i) {
        const int ks = (i + wc + 2 * wr) & 3;
        aoff[i] = (unsigned)((64 * wr + r) * 128 + (((2 * ks + hh) ^ ((r >> 1) & 7)) << 4));
        boff[i] = (unsigned)(16384 + ks * 4096 + (8 * (g4 >> 1) + q4) * 256 + (((4 * wc + 2 * (g4 & 1) + (p4 >> 1)) ^ (q4 << 2)) << 4) + (p4 & 1) * 8);
    }
    f32x16 acc[2] = {};
    float sx[2] = {0.f, 0.f}, sq[2] = {0.f, 0.f}, cs = 0.f;
    const bf16x2 ones = {(__bf16)1.0f, (__bf16)1.0f};
#pragma unroll
    for (int kt = 0; kt < 10; ++kt) {
        const int ahead = (10 - 1 - kt) < (NST - 2) ? (10 - 1 - kt) : (NST - 2);
        if (ahead >= 3) WAIT_BAR(12); else if (ahead == 2) WAIT_BAR(8); else if (ahead == 1) WAIT_BAR(4); else WAIT_BAR(0);
        if (kt == 0) STAMP(1);
        if (kt + NST - 1 < 10) stage(kt + NST - 1);
        lptr sb = lds + (kt % NST) * 32768;
        bf16x8 a0[4], a1[4], b[4];
#pragma unroll
        for (int i = 0; i < 4; ++i) {
            b[i] = cat8(lds_tr(sb + boff[i]), lds_tr(sb + boff[i] + 1024));
            a0[i] = lds_b128(sb + aoff[i]); a1[i] = lds_b128(sb + aoff[i] + 4096);
        }
#pragma unroll
        for (int i = 0; i < 4; ++i) {
            if (MODE == 2) { acc[0] = MFMA32(a0[i], b[i], acc[0]); acc[1] = MFMA32(a1[i], b[i], acc[1]); }
            else           { acc[0] = MFMA32(b[i], a0[i], acc[0]); acc[1] = MFMA32(b[i], a1[i], acc[1]); }
            if (i == 0) {
#pragma unroll
                for (int e = 0; e < 4; ++e) {
                    const bf16x2 p0 = {a0[0][2 * e], a0[0][2 * e + 1]}, p1 = {a1[0][2 * e], a1[0][2 * e + 1]};
                    sx[0] = dot2(p0, ones, sx[0]); sq[0] = dot2(p0, p0, sq[0]);
                    sx[1] = dot2(p1, ones, sx[1]); sq[1] = dot2(p1, p1, sq[1]);
                }
            }
            if (i < 2) {
#pragma unroll
                for (int e = 0; e < 4; ++e) { const bf16x2 p = {b[i][2 * e], b[i][2 * e + 1]}; cs = dot2(p, ones, cs); }
            }
        }
        asm volatile("" : "+v"(sx[0]), "+v"(sx[1]), "+v"(sq[0]), "+v"(sq[1]), "+v"(cs));
    }
    STAMP(2);
    asm volatile("s_waitcnt lgkmcnt(0)\n\ts_barrier" ::: "memory");
    LAS f32x2* pst = (LAS f32x2*)lds;
    LAS float* pcs = (LAS float*)(lds + 8192);
    LAS f32x2* fst = (LAS f32x2*)(lds + 8192 + 2048);
    LAS float* fcs = (LAS float*)(lds + 8192 + 2048 + 1024);
#pragma unroll
    for (int mt = 0; mt < 2; ++mt) pst[(wc * 2 + hh) * 128 + 64 * wr + 32 * mt + r] = (f32x2){sx[mt], sq[mt]};
    pcs[(wr * 2 + hh) * 128 + 32 * wc + r] = cs;
    __syncthreads();
    if (tid < 128) {
        f32x2 t = pst[tid];
#pragma unroll
        for (int i = 1; i < 8; ++i) t += pst[i * 128 + tid];
        const float mean = t.x * (1.0f / 640.0f);
        const float var = fmaxf(t.y * (1.0f / 640.0f) - mean * mean, 0.f);
        float a = __builtin_amdgcn_rsqf(var + 1e-5f);
        if (MODE == 0) a *= QSCALE;
        if (MODE == 3) a *= __builtin_amdgcn_rsqf(var * a * a + 1e-5f);
        fst[tid] = (f32x2){a, -a * mean};
    } else if (tid < 256) {
        const int c = tid - 128;
        fcs[c] = (pcs[c] + pcs[128 + c]) + (pcs[256 + c] + pcs[384 + c]);
    }
    __syncthreads();
    STAMP(3);
    const int tt0 = (row0 >> 5) + 2 * wr;
    if (MODE == 2) {
        const int cc = col0 + 32 * wc + r, head = cc / 20, d = cc - head * 20;
        const float c = fcs[32 * wc + r];
#pragma unroll
        for (int mt = 0; mt < 2; ++mt) {
            char* dst = ws + WS_KV + (size_t)(head * 64 + tt0 + mt) * KVT + QT + (hh * 21 + d) * 16;
#pragma unroll
            for (int s = 0; s < 2; ++s) {
                f32x8 v;
#pragma unroll
                for (int j = 0; j < 8; ++j) {
                    const f32x2 ab = fst[64 * wr + 32 * mt + 16 * s + 8 * (j >> 2) + 4 * hh + (j & 3)];
                    v[j] = __builtin_fmaf(ab.x, acc[mt][8 * s + j], opq(ab.y) * c);
                }
                *(bf16x8*)(dst + s * 672) = __builtin_convertvector(v, bf16x8);
                if (d == 19) *(u32x4*)(dst + s * 672 + 16) = (u32x4){0x3f803f80u, 0x3f803f80u, 0x3f803f80u, 0x3f803f80u};
            }
        }
    } else {
        f32x4 c4[4];
#pragma unroll
        for (int g = 0; g < 4; ++g) c4[g] = *(const LAS f32x4*)(fcs + 32 * wc + 8 * g + 4 * hh);
        if (MODE == 3) {
            f32x4 bb[4];
#pragma unroll
            for (int g = 0; g < 4; ++g) bb[g] = __builtin_convertvector(*(const bf16x4*)(b1 + col0 + 32 * wc + 8 * g + 4 * hh), f32x4);
#pragma unroll
            for (int mt = 0; mt < 2; ++mt) {
                f32x2 ab = fst[64 * wr + 32 * mt + r]; const float aby = opq(ab.y);
#pragma unroll
                for (int s = 0; s < 2; ++s) {
                    f32x8 v;
#pragma unroll
                    for (int j = 0; j < 8; ++j) {
                        const int g = 2 * s + (j >> 2);
                        v[j] = gelu_tanh(__builtin_fmaf(ab.x, acc[mt][8 * s + j], __builtin_fmaf(aby, c4[g][j & 3], bb[g][j & 3])));
                    }
                    *(bf16x8*)(ws + WS_AF + ((size_t)(tt0 + mt) * NKS + ((col0 + 32 * wc) >> 4) + s) * 1024 + l * 16) = __builtin_convertvector(v, bf16x8);
                }
            }
        } else {
#pragma unroll
            for (int g = 0; g < 4; ++g) {
                const int qi = ((col0 + 32 * wc) >> 2) + 2 * g + hh, head = qi / 5, qd = qi - head * 5;
                const int off = qd < 4 ? (qd >> 1) * 512 + r * 16 + (qd & 1) * 8 : 1024 + r * 8;
#pragma unroll
                for (int mt = 0; mt < 2; ++mt) {
                    const f32x2 ab = fst[64 * wr + 32 * mt + r]; const float aby = opq(ab.y);
                    f32x4 v;
#pragma unroll
                    for (int j = 0; j < 4; ++j) v[j] = __builtin_fmaf(ab.x, acc[mt][4 * g + j], aby * c4[g][j]);
                    char* dst = MODE == 0 ? ws + WS_Q + (size_t)(head * 64 + tt0 + mt) * QT + off : ws + WS_KV + (size_t)(head * 64 + tt0 + mt) * KVT + off;
                    *(bf16x4*)dst = __builtin_convertvector(v, bf16x4);
                }
            }
        }
    }
    STAMP(4); STAMP_FLUSH(0);
}

__global__ __launch_bounds__(512, 4) void k_gemm1(const char* __restrict__ x, const char* __restrict__ wqkv, const char* __restrict__ w1,
                                                  const __bf16* __restrict__ b1, char* __restrict__ ws) {
    __shared__ __attribute__((aligned(16))) char smem[65536];
    lptr lds = (lptr)smem;
    const int b = blockIdx.x, xcd = b & 7, i = b >> 3;
    const int rp = 2 * xcd + (i & 1), ct = i >> 1;
    if (ct < 5) gemm1_tile<0, 2>(x, wqkv, 1920, rp * 128, ct * 128, b1, ws, lds);
    else if (ct < 10) gemm1_tile<1, 2>(x, wqkv + 1280, 1920, rp * 128, (ct - 5) * 128, b1, ws, lds);
    else if (ct < 15) gemm1_tile<2, 2>(x, wqkv + 2560, 1920, rp * 128, (ct - 10) * 128, b1, ws, lds);
    else gemm1_tile<3, 2>(x, w1, 1536, rp * 128, (ct - 15) * 128, b1, ws, lds);
}

constexpr int ATT_STAGE = 8 * KVT;
DI float hmax16(const f32x16& s) {
    float a = fmaxf(fmaxf(s[0], s[1]), s[2]), b = fmaxf(fmaxf(s[3], s[4]), s[5]);
    a = fmaxf(fmaxf(a, s[6]), s[7]); b = fmaxf(fmaxf(b, s[8]), s[9]);
    a = fmaxf(fmaxf(a, s[10]), s[11]); b = fmaxf(fmaxf(b, s[12]), s[13]);
    return fmaxf(fmaxf(a, b), fmaxf(s[14], s[15]));
}
DI void set_ref(bf16x8& q2, float m, int hh) {
    const float nm = hh ? 0.f : -m;
    const __bf16 a = (__bf16)nm; const float r1 = nm - (float)a;
    const __bf16 b = (__bf16)r1; const __bf16 c = (__bf16)(r1 - (float)b);
    q2[4] = a; q2[5] = b; q2[6] = c; q2[7] = (__bf16)0.f;
}

DI void attn_block(int head, int qb, char* __restrict__ ws, lptr lds) {
    STAMP_DECL;
    STAMP(0);
    const int tid = threadIdx.x, l = tid & 63, r = l & 31, hh = l >> 5;
    const int w = __builtin_amdgcn_readfirstlane(tid >> 6), g = w & 3, kh = w >> 2;
    const unsigned lds0 = (unsigned)(uintptr_t)lds;
    const char* kvh = ws + WS_KV + (size_t)head * 64 * KVT;
    const char* src = kvh + (size_t)(kh * 32 + g) * KVT;
    const unsigned voff = l * 16;
    auto stage = [&](int s, int buf) {
        const unsigned dst = lds0 + (unsigned)buf * ATT_STAGE + (unsigned)w * KVT;
        const char* p = src + (size_t)s * 4 * KVT;
        glds16<0>(p, voff, dst);
        glds16<1024>(p, voff, dst);
        if (l < 36) glds16<2048>(p, voff, dst);
    };
    bf16x8 q1[2], q2[2];
#pragma unroll
    for (int i = 0; i < 2; ++i) {
        const char* qp = ws + WS_Q + (size_t)(head * 64 + qb * 8 + 2 * g + i) * QT;
        q1[i] = *(const bf16x8*)(qp + hh * 512 + r * 16);
        bf16x4 t = *(const bf16x4*)(qp + 1024 + r * 8);
        if (hh) t = (bf16x4){};
        q2[i] = cat8(t, (bf16x4){});
    }
    const bf16x4 kone = {(__bf16)1.0f, (__bf16)1.0f, (__bf16)1.0f, (__bf16)0.f};
    const unsigned k1off = (unsigned)(kh * 4 * KVT + l * 16);
    const unsigned k2off = (unsigned)(kh * 4 * KVT + 1024 + r * 8);
    const int vr = r <= 20 ? r : (r < 28 ? 20 : 16);
    const unsigned voffl = (unsigned)(kh * 4 * KVT + QT + (hh * 21 + vr) * 16);
    const f32x16 zero = {};
    LAS float* red = (LAS float*)lds;
    float mref[2];

#pragma nounroll
    for (int attempt = 0; attempt < 2; ++attempt) {
        stage(0, 0); stage(1, 1);
        WAIT_BAR(3);
        if (attempt == 0) {
            const bf16x8 k1 = lds_b128(lds + l * 16);
            const bf16x8 k2 = cat8(*(const LAS bf16x4*)(lds + 1024 + r * 8), kone);
#pragma unroll
            for (int i = 0; i < 2; ++i) {
                f32x16 s = MFMA32(k2, q2[i], zero); s = MFMA32(k1, q1[i], s);
                float m = hmax16(s); m = fmaxf(m, swap32_other(m));
                mref[i] = m;
            }
        }
        set_ref(q2[0], mref[0], hh); set_ref(q2[1], mref[1], hh);
        STAMP(1);
        f32x16 o[2] = {};
        f32x16 sa, sb;
        bf16x8 k1 = lds_b128(lds + k1off), k2 = cat8(*(const LAS bf16x4*)(lds + k2off), kone);
        bf16x8 v0 = lds_b128(lds + voffl), v1 = lds_b128(lds + voffl + 672);
        bf16x8 p1 = {};
        bf16x8 v1p = v1;
        sa = MFMA32(k2, q2[0], zero); sa = MFMA32(k1, q1[0], sa);
#define EX(x) __builtin_amdgcn_exp2f(x)
#define CV(P, i, S, j) do { const f32x2 t_ = {S[j], S[j + 1]}; const bf16x2 c_ = __builtin_convertvector(t_, bf16x2); P[2 * (i)] = c_[0]; P[2 * (i) + 1] = c_[1]; } while (0)
#define PIN(x) asm volatile("" : "+v"(x))
#define UNIT(SC, SN, IC, IN, V1PREV, AFTER_D, AFTER_B, AFTER_C) do { bf16x8 p0; \
        SC[0] = EX(SC[0]); SC[1] = EX(SC[1]); PIN(SC); SBAR(); \
        o[IN] = MFMA32(V1PREV, p1, o[IN]); AFTER_D; SBAR(); \
        SC[2] = EX(SC[2]); SC[3] = EX(SC[3]); CV(p0, 0, SC, 0); CV(p0, 1, SC, 2); PIN(p0); SBAR(); \
        SN = MFMA32(k2, q2[IN], zero); SBAR(); \
        SC[4] = EX(SC[4]); SC[5] = EX(SC[5]); SC[6] = EX(SC[6]); SC[7] = EX(SC[7]); CV(p0, 2, SC, 4); CV(p0, 3, SC, 6); PIN(p0); SBAR(); \
        SN = MFMA32(k1, q1[IN], SN); AFTER_B; SBAR(); \
        SC[8] = EX(SC[8]); SC[9] = EX(SC[9]); SC[10] = EX(SC[10]); SC[11] = EX(SC[11]); PIN(SC); SBAR(); \
        o[IC] = MFMA32(v0, p0, o[IC]); AFTER_C; SBAR(); \
        CV(p1, 0, SC, 8); CV(p1, 1, SC, 10); \
        SC[12] = EX(SC[12]); SC[13] = EX(SC[13]); SC[14] = EX(SC[14]); SC[15] = EX(SC[15]); CV(p1, 2, SC, 12); CV(p1, 3, SC, 14); PIN(p1); SBAR(); \
        } while (0)
        int buf = 0;
        for (int s = 0; s < 8; ++s) {
            const int nbuf = buf == 2 ? 0 : buf + 1;
            lptr cb = lds + buf * ATT_STAGE;
            lptr nb = lds + nbuf * ATT_STAGE;
#pragma unroll
            for (int j = 0; j < 4; ++j) {
                if (j == 3) {
                    WAIT_BAR(0);
                    if (s + 2 < 8) stage(s + 2, nbuf == 2 ? 0 : nbuf + 1);
                }
                lptr cur = cb + j * KVT;
                lptr nxt = j == 3 ? nb : cb + (j + 1) * KVT;
                UNIT(sa, sb, 0, 1, v1p, v1 = lds_b128(cur + voffl + 672),
                     k1 = lds_b128(nxt + k1off); k2 = cat8(*(const LAS bf16x4*)(nxt + k2off), kone), (void)0);
                UNIT(sb, sa, 1, 0, v1, (void)0, (void)0, v0 = lds_b128(nxt + voffl));
                v1p = v1;
            }
            buf = nbuf;
        }
        o[1] = MFMA32(v1p, p1, o[1]);
#undef UNIT
#undef CV
#undef EX
        STAMP(2);
        WAIT_BAR(0);
        int tz = threadIdx.x; asm volatile("" : "+v"(tz));
        const int l = tz & 63, r = l & 31, hh = l >> 5;
        if (kh == 1) {
#pragma unroll
            for (int i = 0; i < 2; ++i)
#pragma unroll
                for (int q = 0; q < 3; ++q)
                    *(LAS f32x4*)(lds + 64 + ((g * 2 + i) * 3 + q) * 1024 + l * 16) = (f32x4){o[i][4 * q], o[i][4 * q + 1], o[i][4 * q + 2], o[i][4 * q + 3]};
        }
        if (tz == 0) red[0] = 0.f;
        __syncthreads();
        if (kh == 0) {
            bool bad = false;
#pragma unroll
            for (int i = 0; i < 2; ++i) {
                f32x4 res[3];
#pragma unroll
                for (int q = 0; q < 3; ++q)
                    res[q] = *(const LAS f32x4*)(lds + 64 + ((g * 2 + i) * 3 + q) * 1024 + l * 16) + (f32x4){o[i][4 * q], o[i][4 * q + 1], o[i][4 * q + 2], o[i][4 * q + 3]};
                const float lo = swap32_other(res[2][0]);
                const float lsum = hh ? res[2][0] : lo;
                const float inv = 1.0f / lsum;
                float chk = lsum;
#pragma unroll
                for (int q = 0; q < 3; ++q)
#pragma unroll
                    for (int e = 0; e < 4; ++e) { if (q < 2 || !hh) chk += fabsf(res[q][e]); res[q][e] *= inv; }
                bad |= !(chk < 3.0e38f);
                char* af = ws + WS_AF + ((size_t)(qb * 8 + 2 * g + i) * NKS + 96) * 1024;
                *(bf16x8*)(af + head * 1024 + l * 16) = cat8(__builtin_convertvector(res[0], bf16x4), __builtin_convertvector(res[1], bf16x4));
                if (!hh) *(bf16x4*)(af + (32 + (head >> 2)) * 1024 + (((head >> 1) & 1) * 32 + r) * 16 + (head & 1) * 8) = __builtin_convertvector(res[2], bf16x4);
            }
            if (bad) red[0] = 1.f;
        }
        __syncthreads();
        STAMP(3); STAMP_FLUSH(1);
        if (attempt == 1 || __builtin_amdgcn_readfirstlane(__float_as_uint(red[0])) == 0u) break;
        set_ref(q2[0], 0.f, hh); set_ref(q2[1], 0.f, hh);
        float m0 = -INFINITY, m1 = -INFINITY;
        for (int t = 0; t < 64; ++t) {
            const char* kp = kvh + (size_t)t * KVT;
            const bf16x8 a1 = *(const bf16x8*)(kp + l * 16);
            const bf16x8 a2 = cat8(*(const bf16x4*)(kp + 1024 + r * 8), kone);
            f32x16 s = MFMA32(a2, q2[0], zero); s = MFMA32(a1, q1[0], s); m0 = fmaxf(m0, hmax16(s));
            s = MFMA32(a2, q2[1], zero); s = MFMA32(a1, q1[1], s); m1 = fmaxf(m1, hmax16(s));
        }
        mref[0] = fmaxf(m0, swap32_other(m0)); mref[1] = fmaxf(m1, swap32_other(m1));
        __syncthreads();
    }
}

__global__ __launch_bounds__(512, 4) void k_attn(char* __restrict__ ws) {
    __shared__ __attribute__((aligned(16))) char smem[3 * ATT_STAGE];
    const int b = blockIdx.x;
    attn_block((b & 7) + 8 * ((b >> 3) & 3), b >> 5, ws, (lptr)smem);
}

constexpr int G2_STAGE = 128 * 160, G2_NST = 3;
__global__ __launch_bounds__(512, 4) void k_gemm2(const __bf16* __restrict__ x, const char* __restrict__ wo, const char* __restrict__ w2,
                                                  const __bf16* __restrict__ b2, char* __restrict__ ws, __bf16* __restrict__ out) {
    STAMP_DECL;
    STAMP(0);
    __shared__ __attribute__((aligned(16))) char smem[G2_NST * G2_STAGE];
    lptr lds = (lptr)smem;
    const int tid = threadIdx.x, l = tid & 63;
    const int w = __builtin_amdgcn_readfirstlane(tid >> 6), strip = w & 1, kq = w >> 1;
    const int b = blockIdx.x, nt = (b >> 3) & 7, rb = (b & 7) * 4 + (b >> 6);
    const unsigned lds0 = (unsigned)(uintptr_t)lds;
    unsigned vo[3], vl[3];
#pragma unroll
    for (int i = 0; i < 3; ++i) {
        const int c = (64 * i + l) % 160, rho = c / 10, ch = c - rho * 10;
        vo[i] = (unsigned)(rho * 1280 + ch * 16);
        vl[i] = (unsigned)(((4 * w + 2 * ((rho >> 2) & 1) + (rho >> 3)) * 20 + 16 + (rho & 3)) * 1280 + ch * 16);
    }
    auto stage = [&](int c) {
        const unsigned dst = lds0 + (unsigned)(c % G2_NST) * G2_STAGE + (unsigned)w * 2560u;
        if (c < 16) {
            const char* p = c < 12 ? w2 + (size_t)(128 * c + 16 * w) * 1280 + nt * 160 : wo + (size_t)(8 * (c - 12) + w) * 20 * 1280 + nt * 160;
            glds16<0>(p, vo[0], dst); glds16<0>(p, vo[1], dst + 1024);
            if (l < 32) glds16<0>(p, vo[2], dst + 2048);
        } else {
            const char* p = wo + nt * 160;
            glds16<0>(p, vl[0], dst); glds16<0>(p, vl[1], dst + 1024);
            if (l < 32) glds16<0>(p, vl[2], dst + 2048);
        }
    };
    const int g4 = l >> 4, q4 = (l & 15) >> 2, p4 = l & 3, r16 = l & 15;
    const char* ap = ws + WS_AF + ((size_t)(2 * rb + strip) * NKS + 2 * kq + (g4 >> 1)) * 1024 + ((g4 & 1) * 32 + r16) * 16;
    const unsigned boff = (unsigned)((32 * kq + 16 * (g4 >> 1) + 4 * (g4 & 1) + q4) * 160 + p4 * 8);
    f32x4 acc[2][5] = {};
    u32x4 an[2][2];
    auto aload = [&](int c, u32x4* dst) {
        asm volatile("global_load_dwordx4 %0, %2, off\n\tglobal_load_dwordx4 %1, %2, off offset:256" : "=&v"(dst[0]), "=&v"(dst[1]) : "v"(ap + (size_t)c * 8192) : "memory");
    };
    stage(0); aload(0, an[0]); stage(1); aload(1, an[1]);
#pragma unroll
    for (int c = 0; c < 17; ++c) {
        if (c < 16) WAIT_BAR(5); else WAIT_BAR(0);
        asm volatile("" : "+v"(an[c & 1][0]), "+v"(an[c & 1][1]));
        const bf16x8 a0 = __builtin_bit_cast(bf16x8, an[c & 1][0]), a1 = __builtin_bit_cast(bf16x8, an[c & 1][1]);
        if (c == 0) STAMP(1);
        if (c + 2 < 17) stage(c + 2);
        lptr sb = lds + (c % G2_NST) * G2_STAGE + boff;
#pragma unroll
        for (int n = 0; n < 5; ++n) {
            const bf16x8 bw = cat8(lds_tr(sb + n * 32), lds_tr(sb + n * 32 + 1280));
            acc[0][n] = MFMA16(bw, a0, acc[0][n]);
            acc[1][n] = MFMA16(bw, a1, acc[1][n]);
        }
        SBAR();
        if (c + 2 < 17) aload(c + 2, an[c & 1]);
    }
    STAMP(2);
    asm volatile("s_waitcnt lgkmcnt(0)\n\ts_barrier" ::: "memory");
#pragma unroll
    for (int ti = 0; ti < 10; ++ti) {
        const int own = ti & 3;
        if (own != kq) *(LAS f32x4*)(lds + ((strip * 10 + ti) * 3 + (kq > own ? kq - 1 : kq)) * 1024 + l * 16) = acc[ti / 5][ti % 5];
    }
    __syncthreads();
#pragma unroll
    for (int ti = 0; ti < 10; ++ti) {
        if ((ti & 3) == kq) {
            f32x4 v = acc[ti / 5][ti % 5];
#pragma unroll
            for (int j = 0; j < 3; ++j) v += *(const LAS f32x4*)(lds + ((strip * 10 + ti) * 3 + j) * 1024 + l * 16);
            const int row = rb * 64 + strip * 32 + (ti / 5) * 16 + r16, col = nt * 80 + (ti % 5) * 16 + 4 * g4;
            v += __builtin_convertvector(*(const bf16x4*)(x + (size_t)row * 640 + col), f32x4) + __builtin_convertvector(*(const bf16x4*)(b2 + col), f32x4);
            *(bf16x4*)(out + (size_t)row * 640 + col) = __builtin_convertvector(v, bf16x4);
        }
    }
    STAMP(3); STAMP_FLUSH(2);
}

extern "C" void kernel_launch(void* const* d_in, const int* in_sizes, int n_in, void* d_out, int out_size, void* d_ws, size_t ws_size, hipStream_t stream) {
    const char* x = (const char*)d_in[0];
    const char* wqkv = (const char*)d_in[1];
    const char* wo = (const char*)d_in[2];
    const char* w1 = (const char*)d_in[4];
    const __bf16* b1 = (const __bf16*)d_in[5];
    const char* w2 = (const char*)d_in[6];
    const __bf16* b2 = (const __bf16*)d_in[7];
    char* ws = (char*)d_ws;
    k_gemm1<<<432, 512, 0, stream>>>(x, wqkv, w1, b1, ws);
    k_attn<<<256, 512, 0, stream>>>(ws);
    k_gemm2<<<256, 512, 0, stream>>>((const __bf16*)x, wo, w2, b2, ws, (__bf16*)d_out);
}
```

```cpp
#include <hip/hip_runtime.h>
#include <hip/hip_bf16.h>
#include <stdint.h>

typedef __bf16 bf16x8 __attribute__((ext_vector_type(8)));
typedef __bf16 bf16x4 __attribute__((ext_vector_type(4)));
typedef __bf16 bf16x2 __attribute__((ext_vector_type(2)));
typedef float f32x16 __attribute__((ext_vector_type(16)));
typedef float f32x8 __attribute__((ext_vector_type(8)));
typedef float f32x4 __attribute__((ext_vector_type(4)));
typedef float f32x2 __attribute__((ext_vector_type(2)));
typedef short s16x4 __attribute__((ext_vector_type(4)));
typedef unsigned u32x4 __attribute__((ext_vector_type(4)));
typedef unsigned u32x2 __attribute__((ext_vector_type(2)));
#define LAS __attribute__((address_space(3)))
#define DI __device__ __forceinline__
typedef LAS char* lptr;

constexpr int S_ = 2048, D_ = 640, H_ = 32, F_ = 1536;
constexpr int QT = 1280;
constexpr int KVT = 2592;
constexpr int NKS = 136;
constexpr size_t WS_Q = 0;
constexpr size_t WS_KV = WS_Q + (size_t)H_ * 64 * QT;
constexpr size_t WS_AF = WS_KV + (size_t)H_ * 64 * KVT;
constexpr size_t WS_YF = WS_AF + (size_t)64 * NKS * 1024;
constexpr size_t WS_CNT = WS_YF + (size_t)64 * 40 * 1024;
constexpr size_t WS_W2 = WS_CNT + 8192;
constexpr float QSCALE = 0.22360679774997896f * 1.4426950408889634f;

#define MFMA32(a, b, c) __builtin_amdgcn_mfma_f32_32x32x16_bf16((a), (b), (c), 0, 0, 0)
#define MFMA16(a, b, c) __builtin_amdgcn_mfma_f32_16x16x32_bf16((a), (b), (c), 0, 0, 0)
#define STAMP_DECL
#define STAMP(i)
#define STAMP_FLUSH(K)
#define SBAR() __builtin_amdgcn_sched_barrier(0)
#define WAIT_BAR(N) asm volatile("s_waitcnt vmcnt(" #N ") lgkmcnt(0)\n\ts_barrier" ::: "memory")

template <int OFF>
DI void glds16(const void* sbase, unsigned voff, unsigned lds) {
    unsigned keep;
    asm volatile("s_mov_b32 %0, m0\n\ts_mov_b32 m0, %3\n\ts_nop 2\n\tglobal_load_lds_dwordx4 %1, %2 offset:%4\n\ts_mov_b32 m0, %0"
                 : "=&s"(keep) : "v"(voff), "s"(sbase), "s"(lds), "i"(OFF) : "memory");
}
DI void glds4(const void* p, unsigned lds) {
    unsigned keep;
    asm volatile("s_mov_b32 %0, m0\n\ts_mov_b32 m0, %2\n\ts_nop 2\n\tglobal_load_lds_dword %1, off\n\ts_mov_b32 m0, %0" : "=&s"(keep) : "v"(p), "s"(lds) : "memory");
}
template <int OFF> DI void gld128(u32x4& d, const void* sbase, unsigned voff) {
    asm volatile("global_load_dwordx4 %0, %1, %2 offset:%3" : "=v"(d) : "v"(voff), "s"(sbase), "i"(OFF) : "memory"); }
template <int OFF> DI void gld128_dev(u32x4& d, const void* sbase, unsigned voff) {
    asm volatile("global_load_dwordx4 %0, %1, %2 offset:%3 sc1" : "=v"(d) : "v"(voff), "s"(sbase), "i"(OFF) : "memory"); }
template <int OFF> DI void gld64(u32x2& d, const void* sbase, unsigned voff) {
    asm volatile("global_load_dwordx2 %0, %1, %2 offset:%3" : "=v"(d) : "v"(voff), "s"(sbase), "i"(OFF) : "memory"); }
DI void gst128_dev(void* sbase, unsigned voff, u32x4 v) {
    asm volatile("global_store_dwordx4 %0, %1, %2 sc1\n\ts_nop 1" :: "v"(voff), "v"(v), "s"(sbase) : "memory"); }
DI void gst64_dev(void* sbase, unsigned voff, u32x2 v) {
    asm volatile("global_store_dwordx2 %0, %1, %2 sc1\n\ts_nop 1" :: "v"(voff), "v"(v), "s"(sbase) : "memory"); }
#define WAIT_VM(N) asm volatile("s_waitcnt vmcnt(" #N ")" ::: "memory")
#define PIN(x) asm volatile("" : "+v"(x))
typedef __attribute__((address_space(1))) unsigned gu32;
DI void arrive(char* ws, int idx) { __hip_atomic_fetch_add((gu32*)(ws + WS_CNT + idx * 256), 1u, __ATOMIC_RELAXED, __HIP_MEMORY_SCOPE_AGENT); }
template <bool ACQ> DI void wait_count(char* ws, int idx, unsigned need, int w) {
    if (w == 0) {
        gu32* p = (gu32*)(ws + WS_CNT + idx * 256);
        for (unsigned spins = 0; spins < (1u << 20); ++spins) {
            if ((unsigned)__builtin_amdgcn_readfirstlane(__hip_atomic_load(p, __ATOMIC_RELAXED, __HIP_MEMORY_SCOPE_AGENT)) >= need) break;
            __builtin_amdgcn_s_sleep(2);
        }
        if (ACQ) __builtin_amdgcn_fence(__ATOMIC_ACQUIRE, "agent");
    }
    asm volatile("s_waitcnt vmcnt(0) lgkmcnt(0)\n\ts_barrier" ::: "memory");
}
template <int CTRL> DI float dpp(float x) { return __builtin_bit_cast(float, __builtin_amdgcn_mov_dpp(__builtin_bit_cast(int, x), CTRL, 0xf, 0xf, true)); }
DI float sum16(float v) { v += dpp<0xB1>(v); v += dpp<0x4E>(v); v += dpp<0x124>(v); v += dpp<0x128>(v); return v; }
DI f32x4 bf4_to_f32(u32x2 v) { return (f32x4){__uint_as_float(v.x << 16), __uint_as_float(v.x & 0xffff0000u), __uint_as_float(v.y << 16), __uint_as_float(v.y & 0xffff0000u)}; }
DI bf16x8 lds_b128(lptr p) { return *(const LAS bf16x8*)p; }
DI bf16x4 lds_tr(lptr p) { return __builtin_bit_cast(bf16x4, __builtin_amdgcn_ds_read_tr16_b64_v4i16((LAS s16x4*)p)); }
DI bf16x8 cat8(bf16x4 a, bf16x4 b) { return __builtin_shufflevector(a, b, 0, 1, 2, 3, 4, 5, 6, 7); }
DI float swap32_other(float v) {
    auto r = __builtin_amdgcn_permlane32_swap(__float_as_uint(v), __float_as_uint(v), false, false);
    return (threadIdx.x & 32) ? __uint_as_float(r[0]) : __uint_as_float(r[1]);
}
DI float opq(float v) { asm("" : "+v"(v)); return v; }
DI float dot2(bf16x2 a, bf16x2 b, float c) { return __builtin_amdgcn_fdot2_f32_bf16(a, b, c, false); }
DI float gelu_tanh(float x) {
    const float e = __builtin_amdgcn_exp2f(x * __builtin_fmaf(x * x, -0.044715f * 2.302208198f, -2.302208198f));
    return x * __builtin_amdgcn_rcpf(1.0f + e);
}

struct Retile { u32x4 v[3]; unsigned dst[3]; };
DI void retile_load(Retile& rt, int pt  , const char* __restrict__ w2, const char* __restrict__ wo) {
    const int l = threadIdx.x & 63;
    const int rq = pt >> 1, cq = pt & 1;
    const char* p;
    if (rq < 384) p = w2 + rq * 4 * 1280;
    else { const int kq = rq - 384, ks = kq >> 2, e = kq & 3; p = wo + (ks < 32 ? ks * 20 + 4 * e : (4 * (ks - 32) + 2 * (e & 1) + (e >> 1)) * 20 + 16) * 1280; }
    p += cq * 640;
#pragma unroll
    for (int i = 0; i < 3; ++i) {
        unsigned c = 64 * i + l; if (c >= 160) c -= 64;
        const unsigned rr = c / 10u, ch = c - rr * 10u, t = rr >> 2, row = rr & 3;
        rt.dst[i] = (cq * 4 + t) * (2176 * 160) + rq * 640 + (row * 10 + ch) * 16;
        gld128<0>(rt.v[i], p, row * 1280 + (t * 10 + ch) * 16);
    }
}
DI void retile_store(const Retile& rt, char* __restrict__ ws) {
#pragma unroll
    for (int i = 0; i < 3; ++i) gst128_dev(ws + WS_W2, rt.dst[i], rt.v[i]);
}

#define RET_AT 6
template <int N> DI void wait_bar() {
    static_assert(N >= 0 && N < 64, "vmcnt");
    asm volatile("s_waitcnt vmcnt(%0) lgkmcnt(0)\n\ts_barrier" :: "i"(N) : "memory");
}
template <int NST>
DI void gemm1_loader(int lw, const char* __restrict__ xa, const char* __restrict__ wb, lptr lds) {
    const int l = threadIdx.x & 63;
    const unsigned dst0 = (unsigned)(uintptr_t)lds + (unsigned)lw * 4096u;
    unsigned va[4], vb[4];
#pragma unroll
    for (int i = 0; i < 4; ++i) {
        va[i] = (unsigned)((32 * lw + 8 * i + (l >> 3)) * 1280 + (((l & 7) ^ (4 * (i & 1) + (l >> 4))) << 4));
        vb[i] = (unsigned)((16 * lw + 4 * i + (l >> 4)) * 3840 + (((l & 15) ^ ((l >> 4) << 2)) << 4));
    }
    auto stage = [&](int kt, int buf) {
        const unsigned dst = dst0 + (unsigned)buf * 32768u;
        const char* pa = xa + kt * 128;
        const char* pb = wb + (size_t)kt * 64 * 3840;
#pragma unroll
        for (int i = 0; i < 4; ++i) { glds16<0>(pa, va[i], dst + i * 1024); glds16<0>(pb, vb[i], dst + 16384 + i * 1024); }
    };
    static_assert(NST == 4, "ring");
    stage(0, 0); stage(1, 1); stage(2, 2);
    wait_bar<16>();
    stage(3, 3);
    int buf = 0;
#pragma unroll 1
    for (int kt = 0; kt < 6; ++kt) {
        wait_bar<16>();
        stage(kt + 4, buf);
        buf = (buf + 1) & 3;
    }
    wait_bar<16>(); wait_bar<8>(); wait_bar<0>();
}

template <int MODE, int NST, int DK>
DI void gemm1_tile(int row0, int col0  , char* __restrict__ ws, lptr lds) {
    STAMP_DECL;
    STAMP(0);
    const int tid = threadIdx.x, l = tid & 63, r = l & 31, hh = l >> 5;
    const int w = __builtin_amdgcn_readfirstlane(tid >> 6), wr = w & 1, wc = w >> 1;
    unsigned aoff[4], boff[4];
    const int g4 = l >> 4, q4 = (l & 15) >> 2, p4 = l & 3;
#pragma unroll
    for (int i = 0; i < 4; ++i) {
        const int ks = (i + wc + 2 * wr) & 3;
        aoff[i] = (unsigned)((64 * wr + r) * 128 + (((2 * ks + hh) ^ ((r >> 1) & 7)) << 4));
        boff[i] = (unsigned)(16384 + ks * 4096 + (8 * (g4 >> 1) + q4) * 256 + (((4 * wc + 2 * (g4 & 1) + (p4 >> 1)) ^ (q4 << 2)) << 4) + (p4 & 1) * 8);
    }
    f32x16 acc[2] = {};
    f32x4 csum = {};
    const unsigned selw = ((l & 15) == (g4 & 1)) ? 0x3f803f80u : 0u;
    const bf16x8 sel = __builtin_bit_cast(bf16x8, (u32x4){selw, selw, selw, selw});
    float sx[2] = {0.f, 0.f}, sq[2] = {0.f, 0.f};
    bf16x8 a0[4], a1[4], b[4];
    auto rdb = [&](int kt, int i) {
 lptr sb = lds + (kt % NST) * 32768; b[i] = cat8(lds_tr(sb + boff[i]), lds_tr(sb + boff[i] + 1024)); };
    auto rda = [&](int kt, int i) {
 lptr sb = lds + (kt % NST) * 32768; a0[i] = lds_b128(sb + aoff[i]); a1[i] = lds_b128(sb + aoff[i] + 4096); };
    auto mm0 = [&](int i) {
 acc[0] = MODE == 2 ? MFMA32(a0[i], b[i], acc[0]) : MFMA32(b[i], a0[i], acc[0]); };
    auto mm1 = [&](int i) {
 acc[1] = MODE == 2 ? MFMA32(a1[i], b[i], acc[1]) : MFMA32(b[i], a1[i], acc[1]); };
    auto stats = [&](unsigned u, float& s, float& q) {
        const float lo = __uint_as_float(u << 16), hi = __uint_as_float(u & 0xffff0000u);
        s += lo; q = __builtin_fmaf(lo, lo, q); s += hi; q = __builtin_fmaf(hi, hi, q);
        PIN(s); PIN(q);
    };
#define LBAR() asm volatile("s_waitcnt lgkmcnt(0)\n\ts_barrier" ::: "memory")
    LBAR();
    STAMP(1);
    rdb(0, 0); rda(0, 0); rdb(0, 1); rda(0, 1);
#pragma unroll
    for (int kt = 0; kt < 10; ++kt) {
        const u32x4 s0 = __builtin_bit_cast(u32x4, a0[0]), s1 = __builtin_bit_cast(u32x4, a1[0]);
        SBAR();
        mm0(0); rdb(kt, 2); stats(s0[0], sx[0], sq[0]); SBAR();
        mm1(0); rda(kt, 2); stats(s0[1], sx[0], sq[0]); SBAR();
        mm0(1); rdb(kt, 3); stats(s0[2], sx[0], sq[0]); SBAR();
        mm1(1); rda(kt, 3); stats(s0[3], sx[0], sq[0]); SBAR();
        csum = MFMA16(sel, b[0], csum); csum = MFMA16(sel, b[1], csum); SBAR();
        if (kt < 9) LBAR();
        mm0(2); if (kt < 9) rdb(kt + 1, 0); stats(s1[0], sx[1], sq[1]); SBAR();
        mm1(2); if (kt < 9) rda(kt + 1, 0); stats(s1[1], sx[1], sq[1]); SBAR();
        mm0(3); if (kt < 9) rdb(kt + 1, 1); stats(s1[2], sx[1], sq[1]); SBAR();
        mm1(3); if (kt < 9) rda(kt + 1, 1); stats(s1[3], sx[1], sq[1]); SBAR();
    }
    STAMP(2);
    asm volatile("s_waitcnt lgkmcnt(0)\n\ts_barrier" ::: "memory");
    LAS f32x2* pst = (LAS f32x2*)lds;
    LAS float* pcs = (LAS float*)(lds + 8192);
    LAS f32x2* fst = (LAS f32x2*)(lds + 8192 + 2048);
    LAS float* fcs = (LAS float*)(lds + 8192 + 2048 + 1024);
#pragma unroll
    for (int mt = 0; mt < 2; ++mt) pst[(wc * 2 + hh) * 128 + 64 * wr + 32 * mt + r] = (f32x2){sx[mt], sq[mt]};
    if (l < 16) { pcs[wr * 128 + 32 * wc + l] = csum[0]; pcs[wr * 128 + 32 * wc + 16 + l] = csum[1]; }
    __syncthreads();
    if (tid < 128) {
        f32x2 t = pst[tid];
#pragma unroll
        for (int i = 1; i < 8; ++i) t += pst[i * 128 + tid];
        const float mean = t.x * (1.0f / 640.0f);
        const float var = fmaxf(t.y * (1.0f / 640.0f) - mean * mean, 0.f);
        float a = __builtin_amdgcn_rsqf(var + 1e-5f);
        if (MODE == 0) a *= QSCALE;
        fst[tid] = (f32x2){a, -a * mean};
    } else if (tid < 256) {
        const int c = tid - 128;
        fcs[c] = pcs[c] + pcs[128 + c];
    }
    __syncthreads();
    STAMP(3);
    const int tt0 = (row0 >> 5) + 2 * wr;
    if (MODE == 2) {
        const int cc = col0 + 32 * wc + r, head = cc / 20, d = cc - head * 20;
        const float c = fcs[32 * wc + r];
#pragma unroll
        for (int mt = 0; mt < 2; ++mt) {
            char* dst = ws + WS_KV + (size_t)(head * 64 + tt0 + mt) * KVT + QT + (hh * 20 + d) * 16;
#pragma unroll
            for (int s = 0; s < 2; ++s) {
                f32x8 v;
#pragma unroll
                for (int j = 0; j < 8; ++j) {
                    const f32x2 ab = fst[64 * wr + 32 * mt + 16 * s + 8 * (j >> 2) + 4 * hh + (j & 3)];
                    v[j] = __builtin_fmaf(ab.x, acc[mt][8 * s + j], opq(ab.y) * c);
                }
                *(bf16x8*)(dst + s * 640) = __builtin_convertvector(v, bf16x8);
            }
            if (d == 19) { const unsigned o_ = hh ? 0u : 0x3f803f80u; *(u32x4*)(dst + 1280 - 304 - hh * 304) = (u32x4){o_, o_, o_, o_}; }
        }
    } else {
        f32x4 c4[4];
#pragma unroll
        for (int g = 0; g < 4; ++g) c4[g] = *(const LAS f32x4*)(fcs + 32 * wc + 8 * g + 4 * hh);
        {
#pragma unroll
            for (int g = 0; g < 4; ++g) {
                const int qi = ((col0 + 32 * wc) >> 2) + 2 * g + hh, head = qi / 5, qd = qi - head * 5;
                const int off = qd < 4 ? (qd >> 1) * 512 + r * 16 + (qd & 1) * 8 : 1024 + r * 8;
#pragma unroll
                for (int mt = 0; mt < 2; ++mt) {
                    const f32x2 ab = fst[64 * wr + 32 * mt + r]; const float aby = opq(ab.y);
                    f32x4 v;
#pragma unroll
                    for (int j = 0; j < 4; ++j) v[j] = __builtin_fmaf(ab.x, acc[mt][4 * g + j], aby * c4[g][j]);
                    char* dst = MODE == 0 ? ws + WS_Q + (size_t)(head * 64 + tt0 + mt) * QT + off : ws + WS_KV + (size_t)(head * 64 + tt0 + mt) * KVT + off;
                    *(bf16x4*)dst = __builtin_convertvector(v, bf16x4);
                }
            }
        }
    }
    STAMP(4); STAMP_FLUSH(DK);
}

#define K1_NST 4
DI void ln2_block(const char* __restrict__ x, int row0, char* __restrict__ ws, lptr lds) {
    const int tid = threadIdx.x, l = tid & 63, w = tid >> 6, c0 = l & 15;
    u32x4 raw[4][5];
#pragma unroll
    for (int i = 0; i < 4; ++i)
#pragma unroll
        for (int j = 0; j < 5; ++j) raw[i][j] = *(const u32x4*)(x + (size_t)(row0 + 16 * w + 4 * i + (l >> 4)) * 1280 + (c0 + 16 * j) * 16);
    SBAR();
#pragma unroll
    for (int i = 0; i < 4; ++i) {
        f32x2 v[20], s = {}, q = {};
#pragma unroll
        for (int j = 0; j < 5; ++j)
#pragma unroll
            for (int e = 0; e < 4; ++e) {
                const f32x2 t = {__uint_as_float(raw[i][j][e] << 16), __uint_as_float(raw[i][j][e] & 0xffff0000u)};
                v[4 * j + e] = t; s += t; q = __builtin_elementwise_fma(t, t, q);
            }
        const float mean = sum16(s.x + s.y) * (1.0f / 640.0f);
        const float var = fmaxf(sum16(q.x + q.y) * (1.0f / 640.0f) - mean * mean, 0.f);
        float a = __builtin_amdgcn_rsqf(var + 1e-5f);
        a *= __builtin_amdgcn_rsqf(var * a * a + 1e-5f);
        const float nb = -a * mean;
#pragma unroll
        for (int j = 0; j < 5; ++j) {
            u32x4 o;
#pragma unroll
            for (int e = 0; e < 4; ++e)
                o[e] = __builtin_bit_cast(unsigned, __builtin_convertvector(__builtin_elementwise_fma(v[4 * j + e], (f32x2){a, a}, (f32x2){nb, nb}), bf16x2));
            *(LAS u32x4*)(lds + w * 11520 + (c0 + 16 * j) * 144 + (4 * (i & 1) + (l >> 4)) * 16) = o;
        }
        if (i & 1) {
            const int row = row0 + 16 * w + 4 * (i - 1);
#pragma unroll
            for (int j = 0; j < 10; ++j)
                *(u32x4*)(ws + WS_YF + (size_t)(row >> 5) * 40960 + (8 * j + (l >> 3)) * 512 + ((row & 31) + (l & 7)) * 16) =
                    *(const LAS u32x4*)(lds + w * 11520 + (8 * j + (l >> 3)) * 144 + (l & 7) * 16);
        }
        SBAR();
    }
}

__global__ __launch_bounds__(768, 3) void k_gemm1(const char* __restrict__ x, const char* __restrict__ wqkv, char* __restrict__ ws) {
    __shared__ __attribute__((aligned(16))) char smem[K1_NST * 32768 + 256];
    lptr lds = (lptr)smem;
    const int b = blockIdx.x, xcd = b & 7, i = b >> 3, s = xcd & 1;
    const int rp = (xcd >> 1) * 4 + (i < 28 ? (i & 3) : 2 * s + (i & 1)), ct = i < 28 ? 8 * s + (i >> 2) : (i < 30 ? 7 : 15);
    const int w = __builtin_amdgcn_readfirstlane(threadIdx.x >> 6);
    if (w >= 8) {
        if (ct < 15) gemm1_loader<K1_NST>(w - 8, x + (size_t)rp * 128 * 1280, wqkv + ct * 256, lds);
        return;
    }
    if (ct < 5) gemm1_tile<0, K1_NST, 0>(rp * 128, ct * 128, ws, lds);
    else if (ct < 10) gemm1_tile<1, K1_NST, 0>(rp * 128, (ct - 5) * 128, ws, lds);
    else if (ct < 15) gemm1_tile<2, K1_NST, 0>(rp * 128, (ct - 10) * 128, ws, lds);
    else {
        STAMP_DECL; STAMP(0);
        if (rp == 0 && threadIdx.x < 25) *(unsigned*)(ws + WS_CNT + threadIdx.x * 256) = 0u;
        ln2_block(x, rp * 128, ws, lds);
        STAMP(1); STAMP_FLUSH(2);
    }
}

constexpr int ATT_STAGE = 8 * KVT;
DI float hmax16(const f32x16& s) {
    float a = fmaxf(fmaxf(s[0], s[1]), s[2]), b = fmaxf(fmaxf(s[3], s[4]), s[5]);
    a = fmaxf(fmaxf(a, s[6]), s[7]); b = fmaxf(fmaxf(b, s[8]), s[9]);
    a = fmaxf(fmaxf(a, s[10]), s[11]); b = fmaxf(fmaxf(b, s[12]), s[13]);
    return fmaxf(fmaxf(a, b), fmaxf(s[14], s[15]));
}
DI void set_ref(bf16x4& q2, float m, int hh) {
    const float nm = -m;
    const __bf16 a = (__bf16)nm; const float r1 = nm - (float)a;
    const __bf16 b = (__bf16)r1; const __bf16 c = (__bf16)(r1 - (float)b);
    if (hh) q2 = (bf16x4){a, b, c, (__bf16)0.f};
}
#define MFMA8(a, b, c) __builtin_amdgcn_mfma_f32_32x32x8bf16_1k(__builtin_bit_cast(s16x4, a), __builtin_bit_cast(s16x4, b), (c), 0, 0, 0)
DI bf16x4 lds_b64(lptr p) { return *(const LAS bf16x4*)p; }

DI void attn_block(int head, int qb, char* __restrict__ ws, lptr lds) {
    STAMP_DECL;
    STAMP(0);
    const int tid = threadIdx.x, l = tid & 63, r = l & 31, hh = l >> 5;
    const int w = __builtin_amdgcn_readfirstlane(tid >> 6), g = w & 3, kh = w >> 2;
    const unsigned lds0 = (unsigned)(uintptr_t)lds;
    const char* kvh = ws + WS_KV + (size_t)head * 64 * KVT;
    const char* src = kvh + (size_t)(kh * 32 + g) * KVT;
    const unsigned voff = l * 16;
    auto stage = [&](int s, int buf) {
        const unsigned dst = lds0 + (unsigned)buf * ATT_STAGE + (unsigned)w * KVT;
        const char* p = src + (size_t)s * 4 * KVT;
        glds16<0>(p, voff, dst);
        glds16<1024>(p, voff, dst);
        if (l < 34) glds16<2048>(p, voff, dst);
    };
    __builtin_amdgcn_s_setprio(3);
    u32x4 q1r[2]; u32x2 q2r[2];
    {
        const char* qp = ws + WS_Q + (size_t)(head * 64 + qb * 8 + 2 * g) * QT;
        gld128<0>(q1r[0], qp, voff); gld128<QT>(q1r[1], qp, voff);
        gld64<1024>(q2r[0], qp, r * 8); gld64<1024 + QT>(q2r[1], qp, r * 8);
    }
    stage(0, 0); stage(1, 1); stage(2, 2);
    WAIT_VM(9);
    PIN(q1r[0]); PIN(q1r[1]); PIN(q2r[0]); PIN(q2r[1]);
    bf16x8 q1[2]; bf16x4 q2[2];
#pragma unroll
    for (int i = 0; i < 2; ++i) {
        q1[i] = __builtin_bit_cast(bf16x8, q1r[i]);
        q2[i] = hh ? (bf16x4){} : __builtin_bit_cast(bf16x4, q2r[i]);
    }
    const unsigned k2rel = hh ? 2560 : 1024 + r * 8;
    const unsigned k1off = (unsigned)(kh * 4 * KVT + l * 16);
    const unsigned k2off = (unsigned)(kh * 4 * KVT) + k2rel;
    const unsigned v0off = (unsigned)(kh * 4 * KVT + (r < 20 ? QT + (hh * 20 + r) * 16 : (r == 20 ? 2560 : 2576)));
    const unsigned v1off = v0off + (r < 20 ? 640 : 0);
    const f32x16 zero = {};
    LAS float* red = (LAS float*)lds;
    float mref[2];

#pragma nounroll
    for (int attempt = 0; attempt < 2; ++attempt) {
        if (attempt) { stage(0, 0); stage(1, 1); stage(2, 2); }
        WAIT_BAR(6);
        if (attempt == 0) {
            const bf16x8 k1 = lds_b128(lds + l * 16);
            const bf16x4 k2 = lds_b64(lds + k2rel);
#pragma unroll
            for (int i = 0; i < 2; ++i) {
                f32x16 s = MFMA8(k2, q2[i], zero); s = MFMA32(k1, q1[i], s);
                float m = hmax16(s); m = fmaxf(m, swap32_other(m));
                mref[i] = m;
            }
        }
        set_ref(q2[0], mref[0], hh); set_ref(q2[1], mref[1], hh);
        STAMP(1);
        __builtin_amdgcn_s_setprio(1);
        f32x16 o[2] = {};
        f32x16 sa, sb;
        bf16x8 k1 = lds_b128(lds + k1off); bf16x4 k2 = lds_b64(lds + k2off);
        bf16x8 v0 = lds_b128(lds + v0off), v1 = lds_b128(lds + v1off);
        bf16x8 p1 = {};
        float ta = 0.f, tb = 0.f;
        sa = MFMA8(k2, q2[0], zero); sa = MFMA32(k1, q1[0], sa);
        sb = MFMA8(k2, q2[1], zero);
#define EX(x) __builtin_amdgcn_exp2f(x)
#define CV(P, i, a, b) do { const f32x2 t_ = {a, b}; const bf16x2 c_ = __builtin_convertvector(t_, bf16x2); P[2 * (i)] = c_[0]; P[2 * (i) + 1] = c_[1]; } while (0)
#define GRP(SC, j, PA, ia, PB, ib) do { float e0_ = EX(SC[j]), e1_ = EX(SC[j + 1]); PIN(e0_); PIN(e1_); CV(PA, ia, ta, tb); PIN(PA); SBAR(); \
        ta = EX(SC[j + 2]); tb = EX(SC[j + 3]); PIN(ta); PIN(tb); CV(PB, ib, e0_, e1_); PIN(PB); SBAR(); } while (0)
#define UNIT(SC, SN, IC, IN, FIRST, AFTER_D, AFTER_B, AFTER_C) do { bf16x8 p0; FIRST; \
        GRP(SC, 0, p1, 3, p0, 0);  o[IN] = MFMA32(v1, p1, o[IN]); AFTER_D; SBAR(); \
        GRP(SC, 4, p0, 1, p0, 2);  SN = MFMA32(k1, q1[IN], SN); AFTER_B; SBAR(); \
        GRP(SC, 8, p0, 3, p1, 0);  o[IC] = MFMA32(v0, p0, o[IC]); AFTER_C; SBAR(); \
        GRP(SC, 12, p1, 1, p1, 2); SC = MFMA8(k2, q2[IC], zero); SBAR(); \
        } while (0)
        int buf = 0;
        for (int s = 0; s < 8; ++s) {
            const int nbuf = buf == 2 ? 0 : buf + 1;
            lptr cb = lds + buf * ATT_STAGE;
            lptr nb = lds + nbuf * ATT_STAGE;
#pragma unroll
            for (int j = 0; j < 4; ++j) {
                if (j == 2) __builtin_amdgcn_s_setprio(0);
                lptr cur = cb + j * KVT;
                lptr nxt = j == 3 ? nb : cb + (j + 1) * KVT;
#define SYNC() do { if (j == 3) { if (s < 6) WAIT_BAR(3); else WAIT_BAR(0); __builtin_amdgcn_s_setprio(3); if (s + 3 < 8) stage(s + 3, buf); __builtin_amdgcn_s_setprio(1); } } while (0)
                UNIT(sa, sb, 0, 1, (void)0, v1 = lds_b128(cur + v1off), SYNC(); k2 = lds_b64(nxt + k2off); k1 = lds_b128(nxt + k1off), (void)0);
                UNIT(sb, sa, 1, 0, (void)0, (void)0, (void)0, v0 = lds_b128(nxt + v0off));
#undef SYNC
            }
            buf = nbuf;
        }
        CV(p1, 3, ta, tb);
        o[1] = MFMA32(v1, p1, o[1]);
#undef UNIT
#undef GRP
#undef CV
#undef EX
        STAMP(2);
        __builtin_amdgcn_s_setprio(3);
        WAIT_BAR(0);
        int tz = threadIdx.x; asm volatile("" : "+v"(tz));
        const int l = tz & 63, r = l & 31, hh = l >> 5;
#pragma unroll
        for (int q = 0; q < 3; ++q) {
            f32x4 t;
#pragma unroll
            for (int e = 0; e < 4; ++e) t[e] = kh ? o[0][4 * q + e] : o[1][4 * q + e];
            *(LAS f32x4*)(lds + 64 + (w * 3 + q) * 1024 + l * 16) = t;
        }
        if (tz == 0) red[0] = 0.f;
        __syncthreads();
        {
            f32x4 res[3];
#pragma unroll
            for (int q = 0; q < 3; ++q) {
                res[q] = *(const LAS f32x4*)(lds + 64 + ((w ^ 4) * 3 + q) * 1024 + l * 16);
#pragma unroll
                for (int e = 0; e < 4; ++e) res[q][e] += kh ? o[1][4 * q + e] : o[0][4 * q + e];
            }
            const float lo = swap32_other(res[2][0]);
            const float lsum = hh ? res[2][0] : lo;
            const float inv = __builtin_amdgcn_rcpf(lsum);
            float chk = lsum;
#pragma unroll
            for (int q = 0; q < 3; ++q)
#pragma unroll
                for (int e = 0; e < 4; ++e) { chk += fabsf(res[q][e]); res[q][e] *= inv; }
            char* af = ws + WS_AF + ((size_t)(qb * 8 + 2 * g + kh) * NKS + 96) * 1024;
            gst128_dev(af + head * 1024, l * 16, __builtin_bit_cast(u32x4, cat8(__builtin_convertvector(res[0], bf16x4), __builtin_convertvector(res[1], bf16x4))));
            if (!hh) gst64_dev(af + (32 + (head >> 2)) * 1024 + ((head >> 1) & 1) * 512 + (head & 1) * 8, r * 16, __builtin_bit_cast(u32x2, __builtin_convertvector(res[2], bf16x4)));
            if (!(chk < 1.0e37f)) red[0] = 1.f;
        }
        WAIT_BAR(0);
        STAMP(3);
        if (attempt == 1 || __builtin_amdgcn_readfirstlane(__float_as_uint(red[0])) == 0u) {
            if (tz == 0) arrive(ws, 16 + qb);
            STAMP_FLUSH(1);
            break;
        }
        set_ref(q2[0], 0.f, hh); set_ref(q2[1], 0.f, hh);
        float m0 = -INFINITY, m1 = -INFINITY;
        for (int t = 0; t < 64; ++t) {
            const char* kp = kvh + (size_t)t * KVT;
            const bf16x8 a1 = *(const bf16x8*)(kp + l * 16);
            const bf16x4 a2 = *(const bf16x4*)(kp + 1024 + r * 8);
            f32x16 s = MFMA8(a2, q2[0], zero); s = MFMA32(a1, q1[0], s); m0 = fmaxf(m0, hmax16(s));
            s = MFMA8(a2, q2[1], zero); s = MFMA32(a1, q1[1], s); m1 = fmaxf(m1, hmax16(s));
        }
        mref[0] = fmaxf(m0, swap32_other(m0)); mref[1] = fmaxf(m1, swap32_other(m1));
        __syncthreads();
    }
}

constexpr int G2_STAGE = 128 * 160;
DI void mlp_block(int mb, const char* __restrict__ x, const char* __restrict__ wo, const char* __restrict__ w1, const char* __restrict__ b1, const char* __restrict__ w2,
                  const char* __restrict__ b2, char* __restrict__ ws, char* __restrict__ out, lptr lds) {
    STAMP_DECL;
    STAMP(0);
    __builtin_amdgcn_s_setprio(2);
    const int w = __builtin_amdgcn_readfirstlane(threadIdx.x >> 6);
    const int xcd = mb & 7, bi = mb >> 3;
    const unsigned lds0 = (unsigned)(uintptr_t)lds;
    {
        Retile rt;
        const int pt = w * 256 + mb;
        retile_load(rt, pt < 1088 ? pt : 0, w2, wo);
        const int l = threadIdx.x & 63, hh = l >> 5, g4 = l >> 4, q4 = (l & 15) >> 2, p4 = l & 3;
        const int strip = w & 3, kh = w >> 2, panel = 2 * xcd + (bi & 1), ct = bi >> 1;
        const unsigned l16 = l * 16;
        unsigned vo[3];
#pragma unroll
        for (int i = 0; i < 3; ++i) { const int c = 64 * i + l, rho = c / 12; vo[i] = (unsigned)(rho * 3072 + (c - rho * 12) * 16); }
        const char* wsrc = w1 + (size_t)(16 * w) * 3072 + ct * 192;
        auto stw = [&](int st) {
            const unsigned dst = lds0 + (unsigned)(st % 3) * 24576u + (unsigned)w * 3072u;
            const char* p = wsrc + (size_t)st * 128 * 3072;
            glds16<0>(p, vo[0], dst); glds16<0>(p, vo[1], dst + 1024); glds16<0>(p, vo[2], dst + 2048);
        };
        const char* ysrc = ws + WS_YF + (size_t)(panel * 4 + strip) * 40960 + kh * 4096;
        u32x4 ya[2][4];
        auto lda = [&](int st) {
            const char* p = ysrc + st * 8192;
            gld128<0>(ya[st & 1][0], p, l16); gld128<1024>(ya[st & 1][1], p, l16); gld128<2048>(ya[st & 1][2], p, l16); gld128<3072>(ya[st & 1][3], p, l16);
        };
        u32x2 bias[3][2];
        const char* bsrc = b1 + (ct * 96 + 16 * kh) * 2;
#pragma unroll
        for (int t = 0; t < 3; ++t) { gld64<0>(bias[t][0], bsrc + t * 64, hh * 8); gld64<16>(bias[t][1], bsrc + t * 64, hh * 8); }
        stw(0); lda(0); stw(1); lda(1); stw(2);
        const unsigned boff = (unsigned)((64 * kh + 8 * (g4 >> 1) + q4) * 192 + (g4 & 1) * 32 + p4 * 8);
        f32x16 acc[3] = {};
#pragma unroll
        for (int st = 0; st < 5; ++st) {
            if (st == 0) WAIT_BAR(10); else if (st < 4) WAIT_BAR(7); else WAIT_BAR(0);
            if (st == 0) { STAMP(1); if (pt < 1088) retile_store(rt, ws); }
            if (st == 4 && threadIdx.x == 0) arrive(ws, 24);
#pragma unroll
            for (int i = 0; i < 4; ++i) PIN(ya[st & 1][i]);
            if (st >= 1 && st + 2 < 5) stw(st + 2);
            lptr sb = lds + (st % 3) * 24576 + boff;
#pragma unroll
            for (int i = 0; i < 4; ++i)
#pragma unroll
                for (int t = 0; t < 3; ++t) {
                    const bf16x8 bw = cat8(lds_tr(sb + i * 3072 + t * 64), lds_tr(sb + i * 3072 + t * 64 + 768));
                    acc[t] = MFMA32(bw, __builtin_bit_cast(bf16x8, ya[st & 1][i]), acc[t]);
                }
            SBAR();
            if (st + 2 < 5) lda(st + 2);
        }
        STAMP(2);
        asm volatile("s_waitcnt lgkmcnt(0)\n\ts_barrier" ::: "memory");
#pragma unroll
        for (int t = 0; t < 3; ++t)
#pragma unroll
            for (int q = 0; q < 2; ++q) {
                f32x4 v;
#pragma unroll
                for (int e = 0; e < 4; ++e) v[e] = kh ? acc[t][4 * q + e] : acc[t][8 + 4 * q + e];
                *(LAS f32x4*)(lds + (w * 6 + t * 2 + q) * 1024 + l16) = v;
            }
        __syncthreads();
        char* hdst = ws + WS_AF + ((size_t)(panel * 4 + strip) * NKS + ct * 6 + kh) * 1024;
#pragma unroll
        for (int t = 0; t < 3; ++t) {
            f32x8 v;
#pragma unroll
            for (int q = 0; q < 2; ++q) {
                PIN(bias[t][q]);
                const f32x4 o = *(const LAS f32x4*)(lds + ((w ^ 4) * 6 + t * 2 + q) * 1024 + l16) + bf4_to_f32(bias[t][q]);
#pragma unroll
                for (int e = 0; e < 4; ++e) v[4 * q + e] = gelu_tanh(o[e] + (kh ? acc[t][8 + 4 * q + e] : acc[t][4 * q + e]));
            }
            gst128_dev(hdst + t * 2048, l16, __builtin_bit_cast(u32x4, __builtin_convertvector(v, bf16x8)));
        }
        WAIT_BAR(0);
        if (threadIdx.x == 0) arrive(ws, panel);
        STAMP(3);
    }
    int tz = threadIdx.x; asm volatile("" : "+v"(tz));
    const int l = tz & 63, g4 = l >> 4, q4 = (l & 15) >> 2, p4 = l & 3, r16 = l & 15;
    const int strip = w & 1, kq = w >> 1, rb = 4 * xcd + (bi & 3), nt = bi >> 2;
    const unsigned l16 = l * 16;
    const char* bsrc = ws + WS_W2 + (size_t)nt * (2176 * 160) + w * 2560;
    auto stb = [&](int c) {
        const unsigned dst = lds0 + (unsigned)(c & 3) * G2_STAGE + (unsigned)w * 2560u;
        const char* p = bsrc + c * G2_STAGE;
        glds16<0>(p, l16, dst); glds16<1024>(p, l16, dst);
        if (l < 32) glds16<2048>(p, l16, dst);
    };
    const char* asrc = ws + WS_AF + ((size_t)(2 * rb + strip) * NKS + 2 * kq) * 1024;
    const unsigned aoff = (unsigned)(g4 * 512 + r16 * 16);
    u32x4 an[5][2];
    auto lda = [&](int c, int slot) { const char* p = asrc + c * 8192; gld128_dev<0>(an[slot][0], p, aoff); gld128_dev<256>(an[slot][1], p, aoff); };
    const unsigned boff = (unsigned)((32 * kq + 16 * (g4 >> 1) + 4 * (g4 & 1) + q4) * 160 + p4 * 8);
    const unsigned xoff = (unsigned)(r16 * 1280 + g4 * 8);
    const size_t tbase = (size_t)(rb * 64 + strip * 32) * 1280 + nt * 160;
    u32x2 xi[3], bi2[3];
#pragma unroll
    for (int j = 0; j < 3; ++j) {
        const int ti = (kq + 4 * j) < 10 ? kq + 4 * j : kq, mt = ti >= 5, n = ti - 5 * mt;
        gld64<0>(xi[j], x + tbase + mt * 16 * 1280 + n * 32, xoff);
        gld64<0>(bi2[j], b2 + nt * 160 + n * 32, g4 * 8);
    }
    wait_count<true>(ws, 24, 256, w);
    stb(0); stb(1); stb(2); stb(3);
    wait_count<false>(ws, rb >> 1, 16, w);
    STAMP(4);
    lda(0, 0); lda(1, 1); lda(2, 2); lda(3, 3);
    f32x4 acc[2][5];
#pragma unroll
    for (int j = 0; j < 3; ++j) { PIN(xi[j]); PIN(bi2[j]); }
#pragma unroll
    for (int ti = 0; ti < 10; ++ti) {
        const f32x4 v = bf4_to_f32(xi[ti >> 2]) + bf4_to_f32(bi2[ti >> 2]);
        acc[ti / 5][ti % 5] = (ti & 3) == kq ? v : (f32x4){};
    }
    auto mm = [&](int c, int slot) {
        PIN(an[slot][0]); PIN(an[slot][1]);
        lptr sb = lds + (c & 3) * G2_STAGE + boff;
#pragma unroll
        for (int n = 0; n < 5; ++n) {
            const bf16x8 bw = cat8(lds_tr(sb + n * 32), lds_tr(sb + n * 32 + 1280));
            acc[0][n] = MFMA16(bw, __builtin_bit_cast(bf16x8, an[slot][0]), acc[0][n]);
            acc[1][n] = MFMA16(bw, __builtin_bit_cast(bf16x8, an[slot][1]), acc[1][n]);
        }
        SBAR();
    };
#pragma unroll
    for (int c = 0; c < 12; ++c) {
        if (c == 0) WAIT_VM(6); else if (c == 1) WAIT_BAR(4); else if (c == 2) WAIT_BAR(7); else if (c < 10) WAIT_BAR(10); else if (c == 10) WAIT_BAR(8); else WAIT_BAR(6);
        if (c >= 1) { stb(c + 3); if (c + 3 < 12) lda(c + 3, (c + 3) & 3); }
        mm(c, c & 3);
    }
    WAIT_BAR(6);
    stb(15);
    bf16x8 b12[5];
#pragma unroll
    for (int n = 0; n < 5; ++n) { b12[n] = cat8(lds_tr(lds + boff + n * 32), lds_tr(lds + boff + n * 32 + 1280)); PIN(b12[n]); }
    asm volatile("s_waitcnt lgkmcnt(0)\n\ts_barrier" ::: "memory");
    stb(16);
    STAMP(5);
    wait_count<false>(ws, 16 + xcd, 32, w);
    STAMP(6);
#pragma unroll
    for (int j = 0; j < 5; ++j) lda(12 + j, j);
    WAIT_VM(8);
    PIN(an[0][0]); PIN(an[0][1]);
#pragma unroll
    for (int n = 0; n < 5; ++n) {
        acc[0][n] = MFMA16(b12[n], __builtin_bit_cast(bf16x8, an[0][0]), acc[0][n]);
        acc[1][n] = MFMA16(b12[n], __builtin_bit_cast(bf16x8, an[0][1]), acc[1][n]);
    }
    SBAR();
    WAIT_VM(6); mm(13, 1);
    WAIT_VM(4); mm(14, 2);
    WAIT_VM(2); mm(15, 3);
    WAIT_VM(0); mm(16, 4);
    asm volatile("s_waitcnt lgkmcnt(0)\n\ts_barrier" ::: "memory");
#pragma unroll
    for (int ti = 0; ti < 10; ++ti)
        *(LAS f32x4*)(lds + kq * 20480 + ((strip * 32 + (ti / 5) * 16 + r16) * 20 + (((ti % 5) * 4 + g4) ^ ((r16 >> 1) & 3))) * 16) = acc[ti / 5][ti % 5];
    __syncthreads();
#pragma unroll
    for (int j = 0; j < 3; ++j) {
        const int u = 64 * j + l, row = u / 20, qd = u - row * 20;
        if (u < 160) {
            lptr p = lds + ((8 * w + row) * 20 + (qd ^ ((row >> 1) & 3))) * 16;
            const f32x4 v = (*(const LAS f32x4*)p + *(const LAS f32x4*)(p + 20480)) + (*(const LAS f32x4*)(p + 40960) + *(const LAS f32x4*)(p + 61440));
            *(bf16x4*)(out + (size_t)(rb * 64 + 8 * w + row) * 1280 + nt * 160 + qd * 8) = __builtin_convertvector(v, bf16x4);
        }
    }
    STAMP(7); STAMP_FLUSH(3);
}

__global__ __launch_bounds__(512, 4) void k_main(const char* __restrict__ x, const char* __restrict__ wo, const char* __restrict__ w1, const char* __restrict__ b1,
                                                 const char* __restrict__ w2, const char* __restrict__ b2, char* __restrict__ ws, char* __restrict__ out) {
    __shared__ __attribute__((aligned(16))) char smem[4 * G2_STAGE];
    const int b = blockIdx.x;
    if (b < 256) attn_block((b & 7) + 8 * ((b >> 3) & 3), b >> 5, ws, (lptr)smem);
    else mlp_block(b - 256, x, wo, w1, b1, w2, b2, ws, out, (lptr)smem);
}

extern "C" void kernel_launch(void* const* d_in, const int* in_sizes, int n_in, void* d_out, int out_size, void* d_ws, size_t ws_size, hipStream_t stream) {
    const char* x = (const char*)d_in[0];
    const char* wqkv = (const char*)d_in[1];
    const char* wo = (const char*)d_in[2];
    const char* w1 = (const char*)d_in[4];
    const char* b1 = (const char*)d_in[5];
    const char* w2 = (const char*)d_in[6];
    const char* b2 = (const char*)d_in[7];
    char* ws = (char*)d_ws;
    k_gemm1<<<256, 768, 0, stream>>>(x, wqkv, ws);
    k_main<<<512, 512, 0, stream>>>(x, wo, w1, b1, w2, b2, ws, (char*)d_out);
}
```

```cpp
#include <hip/hip_runtime.h>
#include <hip/hip_bf16.h>
#include <stdint.h>
#include <type_traits>

typedef __bf16 bf16x8 __attribute__((ext_vector_type(8)));
typedef __bf16 bf16x4 __attribute__((ext_vector_type(4)));
typedef __bf16 bf16x2 __attribute__((ext_vector_type(2)));
typedef float f32x16 __attribute__((ext_vector_type(16)));
typedef float f32x8 __attribute__((ext_vector_type(8)));
typedef float f32x4 __attribute__((ext_vector_type(4)));
typedef float f32x2 __attribute__((ext_vector_type(2)));
typedef short s16x4 __attribute__((ext_vector_type(4)));
typedef unsigned u32x4 __attribute__((ext_vector_type(4)));
typedef unsigned u32x2 __attribute__((ext_vector_type(2)));
#define LAS __attribute__((address_space(3)))
#define DI __device__ __forceinline__
typedef LAS char* lptr;
typedef __attribute__((address_space(1))) unsigned gu32;

constexpr int QT = 1280;
constexpr int KVT = 2688;
constexpr int NKS = 136;
constexpr size_t WS_FLAG = 0;
constexpr size_t WS_Q = 131072;
constexpr size_t WS_KV = WS_Q + (size_t)32 * 64 * QT;
constexpr size_t WS_AF = WS_KV + (size_t)32 * 64 * KVT;
constexpr size_t WS_YF = WS_AF + (size_t)64 * NKS * 1024;
constexpr int F_LN = 16;
constexpr int F_QKV = 32;
constexpr int F_H = F_QKV + 240;
constexpr int F_O = F_H + 256;
constexpr float QSCALE = 0.22360679774997896f * 1.4426950408889634f;

constexpr int KV_STAGE = 4 * KVT, KV_HALF = 3 * KV_STAGE;
constexpr int W2_BASE = 65536, W2_STAGE = 128 * 160;
constexpr int CTL = 155648, L_FST = CTL + 512, L_FCS = CTL + 1536, L_DUMMY = CTL + 2048, L_PST = CTL + 2560, L_PCS = L_PST + 4096;
enum { C_QFREE = 0, C_QFULL = 8, C_PART = 12, C_TAG = 20, C_TAGRDY, C_STAT, C_QDONE, C_KVGO, C_YRDY, C_HRDY, C_HDONE, C_ODONE, C_B12, C_LNDONE,
       C_KVFULL = 32  , C_KVFREE = 40, C_WFREE = 48, C_XCH = 52, C_WFULL = 56 };

#define MFMA32(a, b, c) __builtin_amdgcn_mfma_f32_32x32x16_bf16((a), (b), (c), 0, 0, 0)
#define MFMA16(a, b, c) __builtin_amdgcn_mfma_f32_16x16x32_bf16((a), (b), (c), 0, 0, 0)
#define MFMA8(a, b, c) __builtin_amdgcn_mfma_f32_32x32x8bf16_1k(__builtin_bit_cast(s16x4, a), __builtin_bit_cast(s16x4, b), (c), 0, 0, 0)
#define STAMP_DECL
#define STAMP(i)
#define STAMP_FLUSH(K)
#define SBAR() __builtin_amdgcn_sched_barrier(0)
#define CFENCE() asm volatile("" ::: "memory")
#define PIN(x) asm volatile("" : "+v"(x))
#define LBAR() asm volatile("s_waitcnt lgkmcnt(0)\n\ts_barrier" ::: "memory")
DI int lane_id() { unsigned z = 0; asm volatile("" : "+s"(z)); return __builtin_amdgcn_mbcnt_hi(~0u, __builtin_amdgcn_mbcnt_lo(~0u, z)); }
template <int N> DI void wait_vm() { asm volatile("s_waitcnt vmcnt(%0)" :: "i"(N) : "memory"); }
template <int I, int N, class F> DI void static_for(F&& f) { if constexpr (I < N) { f(std::integral_constant<int, I>{}); static_for<I + 1, N>(f); } }

template <int OFF, bool DEV>
DI void glds16(const void* sbase, unsigned voff, unsigned lds) {
    if constexpr (DEV) asm volatile("s_mov_b32 m0, %2\n\ts_nop 3\n\tglobal_load_lds_dwordx4 %0, %1 offset:%3 sc1" :: "v"(voff), "s"(sbase), "s"(lds), "i"(OFF) : "memory", "m0");
    else               asm volatile("s_mov_b32 m0, %2\n\ts_nop 3\n\tglobal_load_lds_dwordx4 %0, %1 offset:%3" :: "v"(voff), "s"(sbase), "s"(lds), "i"(OFF) : "memory", "m0");
}
DI void glds_4k(const void* sbase, unsigned voff, unsigned lds) {
    asm volatile("s_mov_b32 m0, %2\n\ts_nop 3\n\tglobal_load_lds_dwordx4 %0, %1 sc1\n\tglobal_load_lds_dwordx4 %0, %1 offset:1024 sc1\n\t"
                 "global_load_lds_dwordx4 %0, %1 offset:2048 sc1\n\tglobal_load_lds_dwordx4 %0, %1 offset:3072 sc1" :: "v"(voff), "s"(sbase), "s"(lds) : "memory", "m0");
}
DI void touch(const void* sbase, unsigned voff, unsigned lds) {
    asm volatile("s_mov_b32 m0, %2\n\ts_nop 3\n\tglobal_load_lds_dword %0, %1 sc1" :: "v"(voff), "s"(sbase), "s"(lds) : "memory", "m0");
}
template <int OFF> DI void gld128(u32x4& d, const void* sbase, unsigned voff) {
    asm volatile("global_load_dwordx4 %0, %1, %2 offset:%3" : "=v"(d) : "v"(voff), "s"(sbase), "i"(OFF) : "memory"); }
template <int OFF> DI void gld128_dev(u32x4& d, const void* sbase, unsigned voff) {
    asm volatile("global_load_dwordx4 %0, %1, %2 offset:%3 sc1" : "=v"(d) : "v"(voff), "s"(sbase), "i"(OFF) : "memory"); }
template <int OFF> DI void gld128_dev_into(u32x4& d, const void* sbase, unsigned voff) {
    asm volatile("global_load_dwordx4 %0, %1, %2 offset:%3 sc1" : "+v"(d) : "v"(voff), "s"(sbase), "i"(OFF) : "memory"); }
template <int OFF> DI void gld64(u32x2& d, const void* sbase, unsigned voff) {
    asm volatile("global_load_dwordx2 %0, %1, %2 offset:%3" : "=v"(d) : "v"(voff), "s"(sbase), "i"(OFF) : "memory"); }
template <int OFF> DI void gld64_dev(u32x2& d, const void* sbase, unsigned voff) {
    asm volatile("global_load_dwordx2 %0, %1, %2 offset:%3 sc1" : "=v"(d) : "v"(voff), "s"(sbase), "i"(OFF) : "memory"); }
DI void gst128_dev(void* sbase, unsigned voff, u32x4 v) {
    asm volatile("global_store_dwordx4 %0, %1, %2 sc1\n\ts_nop 1" :: "v"(voff), "v"(v), "s"(sbase) : "memory"); }
DI void gst64_dev(void* sbase, unsigned voff, u32x2 v) {
    asm volatile("global_store_dwordx2 %0, %1, %2 sc1\n\ts_nop 1" :: "v"(voff), "v"(v), "s"(sbase) : "memory"); }

DI LAS unsigned* cw(lptr lds, int i) { return (LAS unsigned*)(lds + CTL) + i; }
DI unsigned cw_get(lptr lds, int i) { return __hip_atomic_load(cw(lds, i), __ATOMIC_RELAXED, __HIP_MEMORY_SCOPE_WORKGROUP); }
template <int LANES = 1> DI void cw_set(lptr lds, int i, unsigned v) {
    asm volatile("s_mov_b64 exec, %2\n\tds_write_b32 %0, %1\n\ts_mov_b64 exec, -1" :: "v"((unsigned)(uintptr_t)cw(lds, i)), "v"(v), "i"((1 << LANES) - 1) : "memory");
}
DI unsigned cw_add(lptr lds, int i) {
    unsigned old = 0; CFENCE();
    if (lane_id() == 0) old = __hip_atomic_fetch_add(cw(lds, i), 1u, __ATOMIC_RELAXED, __HIP_MEMORY_SCOPE_WORKGROUP);
    CFENCE();
    return __builtin_amdgcn_readfirstlane(old);
}
template <int N> DI bool cw_ok(lptr lds, int i, unsigned need) { return __all(cw_get(lds, N > 1 ? i + (lane_id() & (N - 1)) : i) >= need); }
template <int N, int SLEEP> DI void cw_wait(lptr lds, int i, unsigned need) {
    const int k = N > 1 ? i + (lane_id() & (N - 1)) : i;
#pragma unroll 1
    for (unsigned spins = 0; spins < (1u << 18); ++spins) { if (__all(cw_get(lds, k) >= need)) break; __builtin_amdgcn_s_sleep(SLEEP); }
    CFENCE();
}
DI void flag_set(char* ws, int idx, unsigned tag) { if (lane_id() == 0) __hip_atomic_store((gu32*)(ws + WS_FLAG) + idx * 32, tag, __ATOMIC_RELAXED, __HIP_MEMORY_SCOPE_AGENT); }
template <int SLEEP> DI void flag_wait(char* ws, int idx  , unsigned tag) {
#pragma unroll 1
    for (unsigned spins = 0; spins < (1u << 17); ++spins) {
        if (__all(__hip_atomic_load((gu32*)(ws + WS_FLAG) + idx * 32, __ATOMIC_RELAXED, __HIP_MEMORY_SCOPE_AGENT) == tag)) break;
        __builtin_amdgcn_s_sleep(SLEEP);
    }
    CFENCE();
}
#define POLL_GAP 4
struct Polls { unsigned v0 = 0, v1 = 0, v2 = 0; DI void release() { asm volatile("" :: "v"(v0), "v"(v1), "v"(v2)); } };
DI void flag_wait_fast(Polls& pl, char* ws, int idx  , unsigned tag) {
    unsigned &v0 = pl.v0, &v1 = pl.v1, &v2 = pl.v2;
    const char* base = ws + WS_FLAG; const unsigned off = idx * 128;
#define POLL(v) asm volatile("global_load_dword %0, %1, %2 sc1" : "=v"(v) : "v"(off), "s"(base) : "memory")
#define SEEN(v) asm volatile("s_waitcnt vmcnt(2)" : "+v"(v) :: "memory"); if (__all(v == tag)) break; __builtin_amdgcn_s_sleep(POLL_GAP); POLL(v)
    wait_vm<0>();
    POLL(v0); __builtin_amdgcn_s_sleep(POLL_GAP); POLL(v1); __builtin_amdgcn_s_sleep(POLL_GAP); POLL(v2);
#pragma unroll 1
    for (unsigned spins = 0; spins < (1u << 16); ++spins) { SEEN(v0); SEEN(v1); SEEN(v2); }
    CFENCE();
#undef POLL
#undef SEEN
}
DI unsigned get_tag(lptr lds) { cw_wait<1, 2>(lds, C_TAGRDY, 1); return __builtin_amdgcn_readfirstlane(cw_get(lds, C_TAG)); }
DI void publish(lptr lds, int ctr, unsigned n, char* ws, int flag) {
    wait_vm<0>();
    if (cw_add(lds, ctr) == n - 1) flag_set(ws, flag, get_tag(lds));
}

template <int CTRL> DI float dpp(float x) { return __builtin_bit_cast(float, __builtin_amdgcn_mov_dpp(__builtin_bit_cast(int, x), CTRL, 0xf, 0xf, true)); }
DI float sum16(float v) { v += dpp<0xB1>(v); v += dpp<0x4E>(v); v += dpp<0x124>(v); v += dpp<0x128>(v); return v; }
DI f32x4 bf4_to_f32(u32x2 v) { return (f32x4){__uint_as_float(v.x << 16), __uint_as_float(v.x & 0xffff0000u), __uint_as_float(v.y << 16), __uint_as_float(v.y & 0xffff0000u)}; }
DI bf16x8 lds_b128(lptr p) { return *(const LAS bf16x8*)p; }
DI bf16x4 lds_b64(lptr p) { return *(const LAS bf16x4*)p; }
DI bf16x4 lds_tr(lptr p) { return __builtin_bit_cast(bf16x4, __builtin_amdgcn_ds_read_tr16_b64_v4i16((LAS s16x4*)p)); }
DI bf16x8 cat8(bf16x4 a, bf16x4 b) { return __builtin_shufflevector(a, b, 0, 1, 2, 3, 4, 5, 6, 7); }
DI f32x2 both_halves(float v) {
    auto r = __builtin_amdgcn_permlane32_swap(__float_as_uint(v), __float_as_uint(v), false, false);
    return (f32x2){__uint_as_float(r[0]), __uint_as_float(r[1])};
}
DI float max_halves(float v) { const f32x2 t = both_halves(v); return fmaxf(t.x, t.y); }
DI float opq(float v) { asm("" : "+v"(v)); return v; }
DI unsigned gelu2(float a, float b) {
    const f32x2 x = {a, b};
    const f32x2 z = x * __builtin_elementwise_fma(x * x, (f32x2){-0.044715f * 2.302208198f, -0.044715f * 2.302208198f}, (f32x2){-2.302208198f, -2.302208198f});
    const f32x2 d = (f32x2){__builtin_amdgcn_exp2f(z.x), __builtin_amdgcn_exp2f(z.y)} + 1.0f;
    const f32x2 y = x * (f32x2){__builtin_amdgcn_rcpf(d.x), __builtin_amdgcn_rcpf(d.y)};
    return __builtin_bit_cast(unsigned, __builtin_convertvector(y, bf16x2));
}

struct Args { const char *x, *wqkv, *wo, *w1, *b1, *w2, *b2; char *ws, *out; };

#define QLEAD 9
template <bool ISV>
DI void qkv_tile(int w, int rp, int ct, char* __restrict__ ws, lptr lds) {
    STAMP_DECL;
    STAMP(0);
    const int l = lane_id(), r = l & 31, hh = l >> 5, wr = w & 1, wc = w >> 1;
    const int g4 = l >> 4, q4 = (l & 15) >> 2, p4 = l & 3;
    unsigned aoff[4], boff[4];
#pragma unroll
    for (int i = 0; i < 4; ++i) {
        const int ks = (i + wc + 2 * wr) & 3;
        aoff[i] = (unsigned)((64 * wr + r) * 128 + (((2 * ks + hh) ^ ((r >> 1) & 7)) << 4));
        boff[i] = (unsigned)(16384 + ks * 4096 + (8 * (g4 >> 1) + q4) * 256 + (((4 * wc + 2 * (g4 & 1) + (p4 >> 1)) ^ (q4 << 2)) << 4) + (p4 & 1) * 8);
    }
    f32x16 acc[2] = {};
    f32x4 csum = {};
    const unsigned selw = ((l & 15) == (g4 & 1)) ? 0x3f803f80u : 0u;
    const bf16x8 sel = __builtin_bit_cast(bf16x8, (u32x4){selw, selw, selw, selw});
    float sx[2] = {0.f, 0.f}, sq[2] = {0.f, 0.f};
    bf16x8 a0[4], a1[4], b[4];
    auto rdb = [&](int h, int i) { b[i] = cat8(lds_tr(lds + boff[i] + h * 32768), lds_tr(lds + boff[i] + h * 32768 + 1024)); };
    auto rda = [&](int h, int i) { a0[i] = lds_b128(lds + aoff[i] + h * 32768); a1[i] = lds_b128(lds + aoff[i] + h * 32768 + 4096); };
    auto mm0 = [&](int i) {
 acc[0] = ISV ? MFMA32(a0[i], b[i], acc[0]) : MFMA32(b[i], a0[i], acc[0]); PIN(acc[0]); };
    auto mm1 = [&](int i) {
 acc[1] = ISV ? MFMA32(a1[i], b[i], acc[1]) : MFMA32(b[i], a1[i], acc[1]); PIN(acc[1]); };
    auto stats = [&](unsigned u, float& s, float& q) {
        const float lo = __uint_as_float(u << 16), hi = __uint_as_float(u & 0xffff0000u);
        s += lo; q = __builtin_fmaf(lo, lo, q); s += hi; q = __builtin_fmaf(hi, hi, q);
        PIN(s); PIN(q);
    };
    const int qword = (l & 8) ? C_QFULL + (l & 3) : C_QFREE + (l & 7);
    const unsigned qlead = (l & 8) ? 0 : QLEAD;
    cw_wait<4, 1>(lds, C_QFULL, 1);
    STAMP(1);
    rdb(0, 0); rda(0, 0); rdb(0, 1); rda(0, 1);
#pragma unroll 1
    for (int k2 = 0; k2 < 10; k2 += 2) {
#pragma unroll
        for (int h = 0; h < 2; ++h) {
            const u32x4 s0 = __builtin_bit_cast(u32x4, a0[0]), s1 = __builtin_bit_cast(u32x4, a1[0]);
            SBAR();
            mm0(0); rdb(h, 2); stats(s0[0], sx[0], sq[0]); SBAR();
            mm1(0); rda(h, 2); stats(s0[1], sx[0], sq[0]); SBAR();
            mm0(1); rdb(h, 3); stats(s0[2], sx[0], sq[0]); SBAR();
            mm1(1); rda(h, 3); cw_set(lds, C_QFREE + w, k2 + h + 1);
            const unsigned full = cw_get(lds, qword);
            stats(s0[3], sx[0], sq[0]); SBAR();
            csum = MFMA16(sel, b[0], csum); csum = MFMA16(sel, b[1], csum); PIN(csum);
            if (h == 1) {
#pragma unroll
                for (int i = 0; i < 4; ++i) { aoff[i] ^= 65536u; boff[i] ^= 65536u; }
            }
            SBAR();
            mm0(2); stats(s1[0], sx[1], sq[1]); SBAR();
            const unsigned need = k2 + h + 2 > 10 ? 10 : k2 + h + 2;
            if (!__all(full + qlead >= need)) {
#pragma unroll 1
                for (unsigned spins = 0; spins < (1u << 18); ++spins) { if (__all(cw_get(lds, qword) + qlead >= need)) break; __builtin_amdgcn_s_sleep(1); }
            }
            CFENCE();
            rdb(h ^ 1, 0); mm1(2); rda(h ^ 1, 0); stats(s1[1], sx[1], sq[1]); SBAR();
            mm0(3); rdb(h ^ 1, 1); stats(s1[2], sx[1], sq[1]); SBAR();
            mm1(3); rda(h ^ 1, 1); stats(s1[3], sx[1], sq[1]); SBAR();
        }
    }
#pragma unroll
    for (int mt = 0; mt < 2; ++mt) {
        const f32x2 ts = both_halves(sx[mt]), tq = both_halves(sq[mt]);
        *(LAS f32x2*)(lds + L_PST + (wc * 128 + 64 * wr + 32 * mt + r) * 8) = (f32x2){ts.x + opq(ts.y), tq.x + opq(tq.y)};
    }
    if (l < 16) { *(LAS float*)(lds + L_PCS + (wr * 128 + 32 * wc + l) * 4) = csum[0]; *(LAS float*)(lds + L_PCS + (wr * 128 + 32 * wc + 16 + l) * 4) = csum[1]; }
    cw_set(lds, C_PART + w, 1);
    STAMP(2);
    __builtin_amdgcn_s_setprio(3);
    cw_wait<1, 1>(lds, C_STAT, 3);
    STAMP(3);
    const LAS f32x2* fst = (const LAS f32x2*)(lds + L_FST);
    const LAS float* fcs = (const LAS float*)(lds + L_FCS);
    const int tt0 = rp * 4 + 2 * wr;
    if (ISV) {
        const int cc = (ct - 10) * 128 + 32 * wc + r, head = cc / 20, d = cc - head * 20;
        const float c = fcs[32 * wc + r];
        const unsigned off = (unsigned)(head * 64 * KVT + QT + (hh * 20 + d) * 16);
#pragma unroll
        for (int mt = 0; mt < 2; ++mt) {
            char* dst = ws + WS_KV + (size_t)(tt0 + mt) * KVT;
#pragma unroll
            for (int s = 0; s < 2; ++s) {
                f32x8 v;
#pragma unroll
                for (int j = 0; j < 8; ++j) {
                    const f32x2 ab = fst[64 * wr + 32 * mt + 16 * s + 8 * (j >> 2) + 4 * hh + (j & 3)];
                    v[j] = __builtin_fmaf(ab.x, acc[mt][8 * s + j], opq(ab.y) * c);
                }
                gst128_dev(dst + s * 640, off, __builtin_bit_cast(u32x4, __builtin_convertvector(v, bf16x8)));
            }
            if (d == 19) { const unsigned o_ = hh ? 0u : 0x3f803f80u; gst128_dev(dst, off + 1280 - 304 - hh * 304, (u32x4){o_, o_, o_, o_}); }
        }
    } else {
        const bool isq = ct < 5;
        const int tile = isq ? QT : KVT;
        char* dst = ws + (isq ? WS_Q : WS_KV) + (size_t)tt0 * tile;
        f32x2 ab[2]; float aby[2];
#pragma unroll
        for (int mt = 0; mt < 2; ++mt) { ab[mt] = fst[64 * wr + 32 * mt + r]; aby[mt] = opq(ab[mt].y); }
#pragma unroll
        for (int g = 0; g < 4; ++g) {
            const f32x4 c4 = *(const LAS f32x4*)(fcs + 32 * wc + 8 * g + 4 * hh);
            u32x2 pk[2];
#pragma unroll
            for (int mt = 0; mt < 2; ++mt) {
                f32x4 v;
#pragma unroll
                for (int j = 0; j < 4; ++j) v[j] = __builtin_fmaf(ab[mt].x, acc[mt][4 * g + j], aby[mt] * c4[j]);
                pk[mt] = __builtin_bit_cast(u32x2, __builtin_convertvector(v, bf16x4));
            }
            const int pi = (isq ? ct : ct - 5) * 16 + 4 * wc + g, k2 = pi / 5, e = pi - 5 * k2;
            if (e == 2) {
                char* p = dst + (size_t)(2 * k2) * 64 * tile + 1024;
                gst64_dev(p, hh * 64 * tile + r * 8, pk[0]); gst64_dev(p + tile, hh * 64 * tile + r * 8, pk[1]);
            } else {
                const auto sx = __builtin_amdgcn_permlane32_swap(pk[0].x, pk[1].x, false, false), sy = __builtin_amdgcn_permlane32_swap(pk[0].y, pk[1].y, false, false);
                gst128_dev(dst + (size_t)(2 * k2 + (e > 2)) * 64 * tile + (e > 2 ? e - 3 : e) * 512, hh * tile + r * 16, (u32x4){sx[0], sy[0], sx[1], sy[1]});
            }
        }
    }
    STAMP(4);
    publish(lds, C_QDONE, 8, ws, F_QKV + ct * 16 + rp);
    STAMP(5); STAMP_FLUSH(0);
}

DI void qkv_stats(int mw, bool isq, lptr lds) {
    const int i = (mw & 1) * 64 + lane_id();
    cw_wait<8, 8>(lds, C_QFREE, 9);
    cw_wait<8, 1>(lds, C_PART, 1);
    if (mw < 2) {
        const LAS f32x2* p = (const LAS f32x2*)(lds + L_PST) + i;
        const f32x2 t = (p[0] + p[128]) + (p[256] + p[384]);
        const float mean = t.x * (1.0f / 640.0f);
        const float var = fmaxf(opq(t.y) * (1.0f / 640.0f) - mean * mean, 0.f);
        const float a = __builtin_amdgcn_rsqf(var + 1e-5f) * (isq ? QSCALE : 1.0f);
        *(LAS f32x2*)(lds + L_FST + i * 8) = (f32x2){a, -a * mean};
    } else {
        const LAS f32x2* p = (const LAS f32x2*)(lds + L_PCS) + (i & 63);
        *(LAS f32x2*)(lds + L_FCS + (i & 63) * 8) = p[0] + p[64];
    }
    cw_add(lds, C_STAT);
}

DI void ln_rows(int w, int rp, const char* __restrict__ x, char* __restrict__ ws, lptr lds) {
    const int l = lane_id(), c0 = l & 15, row0 = rp * 128 + 8 * w;
    u32x4 raw[2][5];
#pragma unroll
    for (int i = 0; i < 2; ++i)
#pragma unroll
        for (int j = 0; j < 5; ++j) raw[i][j] = *(const u32x4*)(x + (size_t)(row0 + 4 * i + (l >> 4)) * 1280 + (c0 + 16 * j) * 16);
    SBAR();
#pragma unroll
    for (int i = 0; i < 2; ++i) {
        f32x2 v[20], s = {}, q = {};
#pragma unroll
        for (int j = 0; j < 5; ++j)
#pragma unroll
            for (int e = 0; e < 4; ++e) {
                const f32x2 t = {__uint_as_float(raw[i][j][e] << 16), __uint_as_float(raw[i][j][e] & 0xffff0000u)};
                v[4 * j + e] = t; s += t; q = __builtin_elementwise_fma(t, t, q);
            }
        const float mean = sum16(s.x + s.y) * (1.0f / 640.0f);
        const float var = fmaxf(sum16(q.x + q.y) * (1.0f / 640.0f) - mean * mean, 0.f);
        const float rs = __builtin_amdgcn_rsqf(var + 1e-5f);
        const float a = rs * __builtin_amdgcn_rsqf(var * rs * rs + 1e-5f), nb = -a * mean;
#pragma unroll
        for (int j = 0; j < 5; ++j)
#pragma unroll
            for (int e = 0; e < 4; ++e)
                raw[i][j][e] = __builtin_bit_cast(unsigned, __builtin_convertvector(__builtin_elementwise_fma(v[4 * j + e], (f32x2){a, a}, (f32x2){nb, nb}), bf16x2));
        SBAR();
    }
    char* dst = ws + WS_YF + (size_t)(row0 >> 5) * 40960 + (row0 & 31) * 16;
    lptr stg = lds + w * 6912;
#pragma unroll
    for (int part = 0; part < 2; ++part) {
#pragma unroll
        for (int j = 3 * part; j < (part ? 5 : 3); ++j)
#pragma unroll
            for (int i = 0; i < 2; ++i) *(LAS u32x4*)(stg + (c0 + 16 * (j - 3 * part)) * 144 + (4 * i + (l >> 4)) * 16) = raw[i][j];
#pragma unroll
        for (int j = 0; j < (part ? 4 : 6); ++j)
            gst128_dev(dst + (6 * part + j) * 4096, (l >> 3) * 512 + (l & 7) * 16, *(const LAS u32x4*)(stg + (8 * j + (l >> 3)) * 144 + (l & 7) * 16));
    }
}

DI float hmax16(const f32x16& s) {
    float a = fmaxf(fmaxf(s[0], s[1]), s[2]), b = fmaxf(fmaxf(s[3], s[4]), s[5]);
    a = fmaxf(fmaxf(a, s[6]), s[7]); b = fmaxf(fmaxf(b, s[8]), s[9]);
    a = fmaxf(fmaxf(a, s[10]), s[11]); b = fmaxf(fmaxf(b, s[12]), s[13]);
    return fmaxf(fmaxf(a, b), fmaxf(s[14], s[15]));
}
DI void set_ref(bf16x4& q2, float m, int hh) {
    const float nm = -m;
    const __bf16 a = (__bf16)nm; const float r1 = nm - (float)a;
    const __bf16 b = (__bf16)r1; const __bf16 c = (__bf16)(r1 - (float)b);
    if (hh) q2 = (bf16x4){a, b, c, (__bf16)0.f};
}

DI void attention(int w, int head, int qb, char* __restrict__ ws, lptr lds) {
    STAMP_DECL;
    const int g = w & 3, kh = w >> 2;
    const char* kvh = ws + WS_KV + (size_t)head * 64 * KVT;
    const f32x16 zero = {};
    bf16x8 q1[2]; bf16x4 q2[2];
    f32x16 o[2] = {};
    {
        const int l = lane_id(), r = l & 31, hh = l >> 5;
        const unsigned k2rel = hh ? 2560 : 1024 + r * 8;
        cw_wait<1, 2>(lds, C_KVGO, 1);
        STAMP(0);
        u32x4 q1r[2], k1r; u32x2 q2r[2], k2r;
        const char* qp = ws + WS_Q + (size_t)(head * 64 + qb * 8 + 2 * g) * QT;
        gld128_dev<0>(k1r, kvh + qb * KV_STAGE, l * 16); gld64_dev<0>(k2r, kvh + qb * KV_STAGE, k2rel);
        gld128_dev<0>(q1r[0], qp, l * 16); gld128_dev<QT>(q1r[1], qp, l * 16);
        gld64_dev<1024>(q2r[0], qp, r * 8); gld64_dev<1024 + QT>(q2r[1], qp, r * 8);
        wait_vm<0>();
        PIN(q1r[0]); PIN(q1r[1]); PIN(q2r[0]); PIN(q2r[1]); PIN(k1r); PIN(k2r);
#pragma unroll
        for (int i = 0; i < 2; ++i) {
            q1[i] = __builtin_bit_cast(bf16x8, q1r[i]);
            q2[i] = hh ? (bf16x4){} : __builtin_bit_cast(bf16x4, q2r[i]);
            f32x16 s = MFMA8(k2r, q2[i], zero); s = MFMA32(__builtin_bit_cast(bf16x8, k1r), q1[i], s);
            set_ref(q2[i], max_halves(hmax16(s)), hh);
        }
        const unsigned k1off = (unsigned)(kh * KV_HALF + l * 16);
        const unsigned k2off = (unsigned)(kh * KV_HALF) + k2rel;
        const unsigned v0off = (unsigned)(kh * KV_HALF + (r < 20 ? QT + (hh * 20 + r) * 16 : (r == 20 ? 2560 : 2576)));
        const unsigned v1off = v0off + (r < 20 ? 640 : 0);
        cw_wait<1, 1>(lds, C_KVFULL + w, 1);
        STAMP(1);
        if (kh) __builtin_amdgcn_s_setprio(0); else __builtin_amdgcn_s_setprio(1);
        f32x16 sa, sb;
        bf16x8 k1 = lds_b128(lds + k1off); bf16x4 k2 = lds_b64(lds + k2off);
        bf16x8 v0 = lds_b128(lds + v0off), v1 = lds_b128(lds + v1off);
        bf16x8 p1 = {};
        float ta = 0.f, tb = 0.f;
        sa = MFMA8(k2, q2[0], zero); sa = MFMA32(k1, q1[0], sa);
        sb = MFMA8(k2, q2[1], zero);
#define EX(x) __builtin_amdgcn_exp2f(x)
#define CV(P, i, a, b) do { const f32x2 t_ = {a, b}; const bf16x2 c_ = __builtin_convertvector(t_, bf16x2); P[2 * (i)] = c_[0]; P[2 * (i) + 1] = c_[1]; } while (0)
#define GRP(SC, j, PA, ia, PB, ib) do { float e0_ = EX(SC[j]), e1_ = EX(SC[j + 1]); PIN(e0_); PIN(e1_); CV(PA, ia, ta, tb); PIN(PA); SBAR(); \
        ta = EX(SC[j + 2]); tb = EX(SC[j + 3]); PIN(ta); PIN(tb); CV(PB, ib, e0_, e1_); PIN(PB); SBAR(); } while (0)
#define UNIT(SC, SN, IC, IN, AFTER_D, AFTER_B, AFTER_C) do { bf16x8 p0; \
        GRP(SC, 0, p1, 3, p0, 0);  o[IN] = MFMA32(v1, p1, o[IN]); PIN(o[IN]); AFTER_D; SBAR(); \
        GRP(SC, 4, p0, 1, p0, 2);  SN = MFMA32(k1, q1[IN], SN); PIN(SN); AFTER_B; SBAR(); \
        GRP(SC, 8, p0, 3, p1, 0);  o[IC] = MFMA32(v0, p0, o[IC]); PIN(o[IC]); AFTER_C; SBAR(); \
        GRP(SC, 12, p1, 1, p1, 2); SC = MFMA8(k2, q2[IC], zero); PIN(SC); SBAR(); \
        } while (0)
        int buf = 0;
        for (int s = 0; s < 8; ++s) {
            const int nbuf = buf == 2 ? 0 : buf + 1;
            lptr cb = lds + buf * KV_STAGE;
            lptr nb = lds + nbuf * KV_STAGE;
            unsigned full = 0;
#pragma unroll
            for (int j = 0; j < 4; ++j) {
                lptr cur = cb + j * KVT;
                lptr nxt = j == 3 ? nb : cb + (j + 1) * KVT;
#define REL() do { if (j == 3) { cw_set(lds, C_KVFREE + w, s + 1); full = cw_get(lds, C_KVFULL + w); } } while (0)
#define ACQ() do { if (j == 3) { if (__builtin_amdgcn_readfirstlane(full) < (unsigned)(s + 2) && s < 7) cw_wait<1, 1>(lds, C_KVFULL + w, s + 2); CFENCE(); } } while (0)
                UNIT(sa, sb, 0, 1, v1 = lds_b128(cur + v1off); REL(), ACQ(); k2 = lds_b64(nxt + k2off); k1 = lds_b128(nxt + k1off), (void)0);
                UNIT(sb, sa, 1, 0, (void)0, (void)0, v0 = lds_b128(nxt + v0off));
#undef REL
#undef ACQ
            }
            buf = nbuf;
        }
        CV(p1, 3, ta, tb);
        o[1] = MFMA32(v1, p1, o[1]);
#undef UNIT
#undef GRP
    }
    STAMP(2);
    __builtin_amdgcn_s_setprio(3);
    const int l = lane_id(), r = l & 31, hh = l >> 5;
    if (kh == 0) {
        cw_wait<4, 1>(lds, C_KVFREE, 8);
#pragma unroll
        for (int i = 0; i < 2; ++i)
#pragma unroll
            for (int q = 0; q < 3; ++q)
                *(LAS f32x4*)(lds + ((g * 2 + i) * 3 + q) * 1024 + l * 16) = (f32x4){o[i][4 * q], o[i][4 * q + 1], o[i][4 * q + 2], o[i][4 * q + 3]};
        cw_set(lds, C_XCH + g, 1);
        STAMP(3); STAMP_FLUSH(1);
        return;
    }
    cw_wait<1, 1>(lds, C_XCH + g, 1);
#pragma unroll
    for (int i = 0; i < 2; ++i) {
        f32x4 res[3];
#pragma unroll
        for (int q = 0; q < 3; ++q)
            res[q] = *(const LAS f32x4*)(lds + ((g * 2 + i) * 3 + q) * 1024 + l * 16) + (f32x4){o[i][4 * q], o[i][4 * q + 1], o[i][4 * q + 2], o[i][4 * q + 3]};
        float chk = 0.f;
#pragma unroll
        for (int q = 0; q < 3; ++q)
#pragma unroll
            for (int e = 0; e < 4; ++e) chk += fabsf(res[q][e]);
        if (__any(!(chk < 1.0e37f))) {
            const unsigned k2rel = hh ? 2560 : 1024 + r * 8;
            const unsigned vrel = r < 20 ? QT + (hh * 20 + r) * 16 : (r == 20 ? 2560 : 2576);
            bf16x4 qz = q2[i]; set_ref(qz, 0.f, hh);
            float m = -INFINITY;
#pragma unroll 1
            for (int t = 0; t < 64; ++t) {
                u32x4 a1; u32x2 a2;
                gld128_dev<0>(a1, kvh + t * KVT, l * 16); gld64_dev<0>(a2, kvh + t * KVT, k2rel);
                wait_vm<0>(); PIN(a1); PIN(a2);
                f32x16 s = MFMA8(a2, qz, zero); s = MFMA32(__builtin_bit_cast(bf16x8, a1), q1[i], s); m = fmaxf(m, hmax16(s));
            }
            set_ref(qz, max_halves(m), hh);
            f32x16 of = {};
#pragma unroll 1
            for (int t = 0; t < 64; ++t) {
                u32x4 a1, b0, b1; u32x2 a2;
                gld128_dev<0>(a1, kvh + t * KVT, l * 16); gld64_dev<0>(a2, kvh + t * KVT, k2rel);
                gld128_dev<0>(b0, kvh + t * KVT, vrel); gld128_dev<0>(b1, kvh + t * KVT, vrel + (r < 20 ? 640 : 0));
                wait_vm<0>(); PIN(a1); PIN(a2); PIN(b0); PIN(b1);
                f32x16 s = MFMA8(a2, qz, zero); s = MFMA32(__builtin_bit_cast(bf16x8, a1), q1[i], s);
                bf16x8 pa, pb;
#pragma unroll
                for (int e = 0; e < 4; ++e) { CV(pa, e, EX(s[2 * e]), EX(s[2 * e + 1])); CV(pb, e, EX(s[8 + 2 * e]), EX(s[9 + 2 * e])); }
                of = MFMA32(__builtin_bit_cast(bf16x8, b0), pa, of); of = MFMA32(__builtin_bit_cast(bf16x8, b1), pb, of);
            }
#pragma unroll
            for (int q = 0; q < 3; ++q) res[q] = (f32x4){of[4 * q], of[4 * q + 1], of[4 * q + 2], of[4 * q + 3]};
        }
        const float inv = __builtin_amdgcn_rcpf(opq(both_halves(res[2][0]).y));
#pragma unroll
        for (int q = 0; q < 3; ++q) res[q] *= inv;
        char* af = ws + WS_AF + ((size_t)(qb * 8 + 2 * g + i) * NKS + 96) * 1024;
        gst128_dev(af + head * 1024, l * 16, __builtin_bit_cast(u32x4, cat8(__builtin_convertvector(res[0], bf16x4), __builtin_convertvector(res[1], bf16x4))));
        if (!hh) gst64_dev(af + (32 + (head >> 2)) * 1024 + ((head >> 1) & 1) * 512 + (head & 1) * 8, r * 16, __builtin_bit_cast(u32x2, __builtin_convertvector(res[2], bf16x4)));
    }
#undef CV
#undef EX
    publish(lds, C_ODONE, 4, ws, F_O + qb * 32 + head);
    STAMP(3); STAMP_FLUSH(1);
}

#define HSLEEP 1
#define PACE 5
#define NOPS(n) do { SBAR(); asm volatile("s_nop %0" :: "i"(n)); SBAR(); } while (0)
DI int w1_slot(int st) { return (st == 0 || st == 3) ? 131072 : (st == 2 ? 98304 : 65536); }

__global__ __launch_bounds__(1024, 4) void k_all(Args a) {
    __shared__ __attribute__((aligned(16))) char smem[163840];
    lptr lds = (lptr)smem;
    char* __restrict__ ws = a.ws;
    const int b = blockIdx.x, xcd = b & 7, bi = b >> 3;
    const int w = __builtin_amdgcn_readfirstlane(threadIdx.x >> 6);
    const int rp = (xcd >> 1) * 4 + (bi < 28 ? (bi & 3) : 2 * (xcd & 1) + (bi & 1)), ct = bi < 28 ? 8 * (xcd & 1) + (bi >> 2) : (bi < 30 ? 7 : 15);
    const int head = xcd + 8 * (bi & 3), qb = bi >> 2;
    if (w == 0) *cw(lds, lane_id()) = 0u;
    LBAR();
    if (w == 11) {
        cw_set(lds, C_TAG, __hip_atomic_load((gu32*)(ws + WS_FLAG), __ATOMIC_RELAXED, __HIP_MEMORY_SCOPE_AGENT) + 1u);
        cw_set(lds, C_TAGRDY, 1);
    }
    if (ct == 15) {
        ln_rows(w, rp, a.x, ws, lds);
        wait_vm<0>();
        if (cw_add(lds, C_LNDONE) == 15) {
            flag_set(ws, F_LN + rp, get_tag(lds));
            cw_set<4>(lds, C_QFREE + (lane_id() & 3), 10); cw_set<4>(lds, C_QFREE + 4 + (lane_id() & 3), 10); cw_set(lds, C_QDONE, 8);
        }
    }
    if (w < 8) {
        __builtin_amdgcn_s_setprio(2);
        if (ct < 10) qkv_tile<false>(w, rp, ct, ws, lds);
        else if (ct < 15) qkv_tile<true>(w, rp, ct, ws, lds);
        int wa = w; asm volatile("" : "+s"(wa));
        attention(wa, head, qb, ws, lds);
        return;
    }
    STAMP_DECL;
    STAMP(0);
    const int panel = 4 * (xcd >> 1) + (bi & 3), hct = 8 * (xcd & 1) + (bi >> 2);
    const int rb = 4 * xcd + (bi & 3), nt = bi >> 2;
    const unsigned lds0 = (unsigned)(uintptr_t)lds;
    const int l = lane_id(), g4 = l >> 4, q4 = (l & 15) >> 2, p4 = l & 3, r16 = l & 15;
    const unsigned l16 = l * 16;
    const int w8 = w - 8, strip = w8 & 1, kq = w8 >> 1;
    const unsigned aoff = (unsigned)(g4 * 512 + r16 * 16);
    const unsigned b2off = (unsigned)(W2_BASE + (16 * (g4 >> 1) + 4 * (g4 & 1) + q4) * 160 + p4 * 8);
    f32x4 acc[2][5];
    unsigned vw[5];
    auto w2_offsets = [&](int kind) {
#pragma unroll
        for (int i = 0; i < 5; ++i) {
            const int c = 64 * i + l, rho = c / 10, ch = c - rho * 10, e = (rho >> 2) & 3;
            const int row = kind == 0 ? rho : (kind == 1 ? rho + (rho >> 4) * 4 : (4 * (rho >> 4) + 2 * (e & 1) + (e >> 1)) * 20 + 16 + (rho & 3));
            vw[i] = (unsigned)(row * 1280 + ch * 16);
        }
    };
    auto stb = [&](int c) {
        const int j = w8 & 1;
        const unsigned dst = lds0 + (unsigned)(W2_BASE + (c & 3) * W2_STAGE + j * 10240);
        const int gs = c < 12 ? 32 : (c < 16 ? 40 : 160);
        const char* p = (c < 12 ? a.w2 + (size_t)c * 128 * 1280 : a.wo + (size_t)(c < 16 ? (c - 12) * 160 : 0) * 1280) + (size_t)(2 * j * gs) * 1280 + nt * 160;
#pragma unroll
        for (int h = 0; h < 2; ++h)
#pragma unroll
            for (int i = 0; i < 5; ++i) glds16<0, false>(p + (size_t)h * gs * 1280, vw[i], dst + h * 5120 + i * 1024);
    };
    const unsigned boff = b2off + (unsigned)(kq * 5120);
    bf16x8 b12[5];
    auto load_b12 = [&]() {
        cw_wait<2, 8>(lds, C_WFULL, 18);
#pragma unroll
        for (int n = 0; n < 5; ++n) { b12[n] = cat8(lds_tr(lds + boff + n * 32), lds_tr(lds + boff + n * 32 + 1280)); PIN(b12[n]); }
        cw_add(lds, C_B12);
    };
    if (w8 < 4) {
        __builtin_amdgcn_s_setprio(3);
        if (w8 == 2) {
            cw_wait<8, 8>(lds, C_QFREE, 5);
            flag_wait<16>(ws, F_LN + 4 * (xcd >> 1) + (l & 3), get_tag(lds));
            const char* p = ws + WS_YF + (size_t)(xcd >> 1) * 655360 + bi * 20480;
            touch(p, l * 128, lds0 + L_DUMMY); touch(p + 8192, l * 128, lds0 + L_DUMMY); if (l < 32) touch(p + 16384, l * 128, lds0 + L_DUMMY);
            cw_set(lds, C_YRDY, 1);
        }
        if (ct < 15 && w8 != 2) qkv_stats(w8, ct < 5, lds);
        __builtin_amdgcn_s_setprio(2);
        {
            cw_wait<1, 4>(lds, C_YRDY, 1);
            const char* ysrc = ws + WS_YF + (size_t)(panel * 4 + w8) * 40960;
            unsigned bias[3];
#pragma unroll
            for (int t = 0; t < 3; ++t) asm volatile("global_load_ushort %0, %1, %2 offset:%3" : "=v"(bias[t]) : "v"((l & 31) * 2), "s"(a.b1 + hct * 192), "i"(t * 64) : "memory");
            u32x4 ya[8];
            static_for<0, 8>([&](auto I) { constexpr int i = I; gld128_dev<(i & 3) * 1024>(ya[i], ysrc + (i >> 2) * 4096, l16); });
            const unsigned boff = (unsigned)((8 * (g4 >> 1) + q4) * 192 + (g4 & 1) * 32 + p4 * 8);
            f32x16 hc[3];
            wait_vm<8>();
            const u32x2 one0 = {l < 32 ? 0x3f80u : 0u, 0u};
#pragma unroll
            for (int t = 0; t < 3; ++t) { PIN(bias[t]); hc[t] = MFMA8(((u32x2){bias[t], 0u}), one0, ((f32x16){})); }
#pragma unroll 1
            for (int st = 0; st < 5; ++st) {
                cw_wait<2, 2>(lds, C_WFULL, st + 1);
                if (st == 0) { STAMP(1); }
                lptr sb = lds + w1_slot(st) + boff;
                const char* ynext = ysrc + (st < 4 ? st + 1 : 4) * 8192;
                static_for<0, 8>([&](auto I) {
                    constexpr int i = I;
                    bf16x8 bw[3];
#pragma unroll
                    for (int t = 0; t < 3; ++t) bw[t] = cat8(lds_tr(sb + i * 3072 + t * 64), lds_tr(sb + i * 3072 + t * 64 + 768));
                    if (i == 7) cw_set(lds, C_WFREE + w8, st + 1);
                    wait_vm<7>();
                    PIN(ya[i]);
#pragma unroll
                    for (int t = 0; t < 3; ++t) { hc[t] = MFMA32(bw[t], __builtin_bit_cast(bf16x8, ya[i]), hc[t]); PIN(hc[t]); }
                    SBAR();
                    gld128_dev_into<(i & 3) * 1024>(ya[i], ynext + (i >> 2) * 4096, l16);
                });
            }
            wait_vm<0>();
#pragma unroll
            for (int i = 0; i < 8; ++i) asm volatile("" :: "v"(ya[i]));
            STAMP(2);
            char* hdst = ws + WS_AF + ((size_t)(panel * 4 + w8) * NKS + hct * 6) * 1024;
#pragma unroll
            for (int t = 0; t < 3; ++t)
#pragma unroll
                for (int s = 0; s < 2; ++s) {
                    u32x4 v;
#pragma unroll
                    for (int e = 0; e < 4; ++e) v[e] = gelu2(hc[t][8 * s + 2 * e], hc[t][8 * s + 2 * e + 1]);
                    gst128_dev(hdst + (2 * t + s) * 1024, l16, v);
                }
            publish(lds, C_HDONE, 4, ws, F_H + panel * 16 + hct);
            STAMP(3);
        }
        const char* asrc = ws + WS_AF + ((size_t)(2 * rb + strip) * NKS + 4 * kq) * 1024;
        u32x4 an[3][4];
        cw_wait<1, 8>(lds, C_HRDY, 1);
        STAMP(4);
#pragma unroll
        for (int u = 0; u < 3; ++u) {
            const char* p = asrc + u * 8192;
            gld128_dev<0>(an[u][0], p, aoff); gld128_dev<256>(an[u][1], p, aoff); gld128_dev<2048>(an[u][2], p, aoff); gld128_dev<2304>(an[u][3], p, aoff);
        }
#pragma unroll
        for (int ti = 0; ti < 10; ++ti) acc[ti / 5][ti % 5] = (f32x4){};
#pragma unroll 1
        for (int c3 = 0; c3 < 12; c3 += 3) {
#pragma unroll
            for (int u = 0; u < 3; ++u) {
                const int c = c3 + u;
                cw_wait<2, 4>(lds, C_WFULL, 6 + c);
                wait_vm<8>();
#pragma unroll
                for (int i = 0; i < 4; ++i) PIN(an[u][i]);
                lptr sb = lds + (c & 3) * W2_STAGE + kq * (64 * 160) + b2off;
#pragma unroll
                for (int i = 0; i < 2; ++i) {
                    bf16x8 bw[5];
#pragma unroll
                    for (int n = 0; n < 5; ++n) bw[n] = cat8(lds_tr(sb + i * 5120 + n * 32), lds_tr(sb + i * 5120 + n * 32 + 1280));
                    if (i == 1) cw_set(lds, C_WFREE + w8, 6 + c);
#pragma unroll
                    for (int n = 0; n < 5; ++n) {
                        acc[0][n] = MFMA16(bw[n], __builtin_bit_cast(bf16x8, an[u][2 * i]), acc[0][n]); PIN(acc[0][n]); NOPS(PACE);
                        acc[1][n] = MFMA16(bw[n], __builtin_bit_cast(bf16x8, an[u][2 * i + 1]), acc[1][n]); PIN(acc[1][n]); NOPS(PACE);
                    }
                }
                SBAR();
                const char* p = asrc + (c + 3 < 12 ? c + 3 : 11) * 8192;
                gld128_dev_into<0>(an[u][0], p, aoff); gld128_dev_into<256>(an[u][1], p, aoff); gld128_dev_into<2048>(an[u][2], p, aoff); gld128_dev_into<2304>(an[u][3], p, aoff);
            }
        }
        wait_vm<0>();
#pragma unroll
        for (int i = 0; i < 12; ++i) asm volatile("" :: "v"(an[i >> 2][i & 3]));
    } else {
        const int hw = w8 - 4;
        __builtin_amdgcn_s_setprio(3);
        auto w1_touch = [&]() {
            const int ln = l < 60 ? l : 0, row = ln / 12;
            touch(a.w1 + (size_t)((bi * 4 + hw) * 5) * 3072 + (xcd & 1) * 1536, row * 3072 + (ln - row * 12) * 128, lds0 + L_DUMMY);
        };
        if (ct < 15) {
            const char* xa = a.x + (size_t)rp * 128 * 1280;
            const char* wb = a.wqkv + ct * 256;
            unsigned va[4], vb[4];
#pragma unroll
            for (int i = 0; i < 4; ++i) {
                va[i] = (unsigned)((32 * hw + 8 * i + (l >> 3)) * 1280 + (((l & 7) ^ (4 * (i & 1) + (l >> 4))) << 4));
                vb[i] = (unsigned)((16 * hw + 4 * i + (l >> 4)) * 3840 + (((l & 15) ^ ((l >> 4) << 2)) << 4));
            }
            auto stage = [&](int kt) {
                const unsigned dst = lds0 + (unsigned)hw * 4096u + (unsigned)(kt & 3) * 32768u;
                const char* pa = xa + kt * 128;
                const char* pb = wb + (size_t)kt * 64 * 3840;
#pragma unroll
                for (int i = 0; i < 4; ++i) { glds16<0, false>(pa, va[i], dst + i * 1024); glds16<0, false>(pb, vb[i], dst + 16384 + i * 1024); }
            };
#pragma unroll 1
            for (int kt = 0; kt < 10; ++kt) {
                if (kt >= 3) {
                    wait_vm<16>(); cw_set(lds, C_QFULL + hw, kt - 2);
                    if (!cw_ok<8>(lds, C_QFREE, kt - 3)) {
                        wait_vm<8>(); cw_set(lds, C_QFULL + hw, kt - 1);
                        cw_wait<8, HSLEEP>(lds, C_QFREE, kt - 3);
                    }
                    CFENCE();
                }
                stage(kt);
            }
        } else cw_wait<8, 8>(lds, C_QFREE, 10);
        w1_touch();
        wait_vm<17>(); cw_set(lds, C_QFULL + hw, 8);
        wait_vm<9>(); cw_set(lds, C_QFULL + hw, 9);
        wait_vm<1>(); cw_set(lds, C_QFULL + hw, 10);
        STAMP(1);
        const unsigned tag = get_tag(lds);
        if (hw < 2) {
            const int c0 = head * 20, ta = c0 >> 7, tb = (c0 + 19) >> 7;
            const int kvf = F_QKV + (5 + 5 * (l & 1) + ((l & 2) ? tb : ta)) * 16 + 8 * hw;
            const char* src = ws + WS_KV + (size_t)(head * 64 + hw * 32) * KVT;
            Polls pl;
            if (hw == 1) cw_wait<1, 1>(lds, C_KVGO, 1);
            else {
                cw_wait<1, 2>(lds, C_QDONE, 8);
                flag_wait_fast(pl, ws, (l & 8) ? F_QKV + ((l & 2) ? tb : ta) * 16 + 2 * qb + (l & 1) : kvf + 2 * (l & 4) + qb, tag);
                cw_set(lds, C_KVGO, 1);
            }
            STAMP(2);
            int buf = 0;
#pragma unroll 1
            for (int s = 0; s < 8; ++s) {
                if (s == 1) flag_wait<2>(ws, kvf + ((l >> 2) & 7), tag);
                if (s >= 3) cw_wait<4, 8>(lds, C_KVFREE + 4 * hw, s - 2);
                const unsigned dst = lds0 + (unsigned)(hw * KV_HALF + buf * KV_STAGE);
                const char* p = src + ((s + qb) & 7) * KV_STAGE;
                glds_4k(p, l16, dst); glds_4k(p + 4096, l16, dst + 4096);
                glds16<0, true>(p + 8192, l16, dst + 8192); glds16<1024, true>(p + 8192, l16, dst + 8192);
                if (l < 32) glds16<2048, true>(p + 8192, l16, dst + 8192);
                wait_vm<0>(); cw_set<4>(lds, C_KVFULL + 4 * hw + (l & 3), s + 1);
                buf = buf == 2 ? 0 : buf + 1;
                if (s == 2) {
                    const int u = bi * 2 + hw;
                    touch(a.w2 + u * 30720, l * 128, lds0 + L_DUMMY); touch(a.w2 + u * 30720 + 8192, l * 128, lds0 + L_DUMMY); touch(a.w2 + u * 30720 + 16384, l * 128, lds0 + L_DUMMY);
                    if (l < 48) touch(a.w2 + u * 30720 + 24576, l * 128, lds0 + L_DUMMY);
                    touch(a.wo + u * 12800, l * 128, lds0 + L_DUMMY); if (l < 36) touch(a.wo + u * 12800 + 8192, l * 128, lds0 + L_DUMMY);
                }
            }
            pl.release();
            if (b == 0 && hw == 0) {
#pragma unroll 1
                for (int i = 0; i < 4; ++i) flag_wait<16>(ws, F_H + 64 * i + l, tag);
                flag_set(ws, 0, tag);
            }
        } else {
            const int j = hw - 2;
            __builtin_amdgcn_s_setprio(2);
            unsigned vo[3];
#pragma unroll
            for (int i = 0; i < 3; ++i) { const int c = 64 * i + l, rho = c / 12; vo[i] = (unsigned)(rho * 3072 + (c - rho * 12) * 16); }
            const char* wsrc = a.w1 + (size_t)(64 * j) * 3072 + hct * 192;
            auto stw = [&](int st) {
                const unsigned dst = lds0 + (unsigned)w1_slot(st) + (unsigned)j * 12288u;
                const char* p = wsrc + (size_t)st * 128 * 3072;
#pragma unroll
                for (int h = 0; h < 4; ++h)
#pragma unroll
                    for (int i = 0; i < 3; ++i) glds16<0, false>(p + h * 16 * 3072, vo[i], dst + h * 3072 + i * 1024);
            };
#pragma unroll 1
            for (int st = 0; st < 5; ++st) {
                const int word = st < 3 ? C_QFREE + (l & 7) : C_WFREE + (l & 3); const unsigned need = st == 0 ? 0 : (st < 3 ? st + 6 : st - 2);
                if (!__all(cw_get(lds, word) >= need)) { wait_vm<0>(); cw_set(lds, C_WFULL + j, st); cw_wait<1, 2>(lds, word, need); }
                CFENCE();
                stw(st);
                if (st >= 2) { wait_vm<24>(); if (__builtin_amdgcn_readfirstlane(cw_get(lds, C_WFULL + j)) < 1u) cw_set(lds, C_WFULL + j, 1); wait_vm<12>(); cw_set(lds, C_WFULL + j, st); }
            }
            wait_vm<0>(); cw_set(lds, C_WFULL + j, 5);
            STAMP(2);
#pragma unroll 1
            for (int c = 0; c < 17; ++c) {
                if (c == 0 || c == 12 || c == 16) w2_offsets(c == 0 ? 0 : (c == 12 ? 1 : 2));
                if (c == 16 || !cw_ok<4>(lds, C_WFREE, c < 3 ? 5 : 2 + c)) {
                    wait_vm<0>(); cw_set(lds, C_WFULL + j, 5 + c);
                    if (c == 4 && j == 1) { flag_wait<8>(ws, F_H + (rb >> 1) * 16 + (l & 15), tag); cw_set(lds, C_HRDY, 1); }
                    if (c == 16) { load_b12(); cw_wait<1, 4>(lds, C_B12, 8); }
                    else cw_wait<4, 4>(lds, C_WFREE, c < 3 ? 5 : 2 + c);
                }
                CFENCE();
                stb(c);
                if (c >= 1) { wait_vm<10>(); cw_set(lds, C_WFULL + j, 5 + c); }
            }
        }
#pragma unroll
        for (int ti = 0; ti < 10; ++ti) acc[ti / 5][ti % 5] = (f32x4){};
    }
    if (w8 < 6) load_b12();
    u32x2 xi[3], bi2[3];
    unsigned eoff[3];
#pragma unroll
    for (int j = 0; j < 3; ++j) {
        const int u = (64 * j + l) < 160 ? 64 * j + l : l, row = u / 20, qd = u - row * 20;
        eoff[j] = (unsigned)(row * 1280 + qd * 8);
        gld64<0>(xi[j], a.x + (size_t)(rb * 64 + 8 * w8) * 1280 + nt * 160, eoff[j]);
        gld64<0>(bi2[j], a.b2 + nt * 160, qd * 8);
    }
    STAMP(5);
    Polls pl;
    wait_vm<0>();
    if (w8 == 4) {
        cw_wait<1, 4>(lds, C_ODONE, 1);
        flag_wait_fast(pl, ws, F_O + xcd * 32 + (l & 31), get_tag(lds));
    }
    LBAR();
    STAMP(6);
    const char* osrc = ws + WS_AF + ((size_t)(2 * rb + strip) * NKS + 96 + 2 * kq) * 1024;
    u32x4 on[5][2];
#pragma unroll
    for (int j = 0; j < 5; ++j) { gld128_dev<0>(on[j][0], osrc + j * 8192, aoff); gld128_dev<256>(on[j][1], osrc + j * 8192, aoff); }
    static_for<0, 5>([&](auto J) {
        constexpr int j = J;
        wait_vm<8 - 2 * j>();
        if (j == 0) pl.release();
        PIN(on[j][0]); PIN(on[j][1]);
        lptr sb = lds + (j & 3) * W2_STAGE + boff;
#pragma unroll
        for (int n = 0; n < 5; ++n) {
            const bf16x8 bw = j == 0 ? b12[n] : cat8(lds_tr(sb + n * 32), lds_tr(sb + n * 32 + 1280));
            acc[0][n] = MFMA16(bw, __builtin_bit_cast(bf16x8, on[j][0]), acc[0][n]);
            acc[1][n] = MFMA16(bw, __builtin_bit_cast(bf16x8, on[j][1]), acc[1][n]);
        }
        SBAR();
    });
    LBAR();
#pragma unroll
    for (int ti = 0; ti < 10; ++ti)
        *(LAS f32x4*)(lds + kq * 20480 + ((strip * 32 + (ti / 5) * 16 + r16) * 20 + (((ti % 5) * 4 + g4) ^ ((r16 >> 1) & 3))) * 16) = acc[ti / 5][ti % 5];
    LBAR();
#pragma unroll
    for (int j = 0; j < 3; ++j) {
        const int u = 64 * j + l, row = u / 20, qd = u - row * 20;
        if (u < 160) {
            PIN(xi[j]); PIN(bi2[j]);
            lptr p = lds + ((8 * w8 + row) * 20 + (qd ^ ((row >> 1) & 3))) * 16;
            const f32x4 v = (*(const LAS f32x4*)p + *(const LAS f32x4*)(p + 20480)) + (*(const LAS f32x4*)(p + 40960) + *(const LAS f32x4*)(p + 61440)) + (bf4_to_f32(xi[j]) + bf4_to_f32(bi2[j]));
            *(bf16x4*)(a.out + (size_t)(rb * 64 + 8 * w8) * 1280 + nt * 160 + eoff[j]) = __builtin_convertvector(v, bf16x4);
        }
    }
    STAMP(7);
    if (w8 < 4) { STAMP_FLUSH(2); } else { STAMP_FLUSH(3); }
}

extern "C" void kernel_launch(void* const* d_in, const int* in_sizes, int n_in, void* d_out, int out_size, void* d_ws, size_t ws_size, hipStream_t stream) {
    Args a;
    a.x = (const char*)d_in[0]; a.wqkv = (const char*)d_in[1]; a.wo = (const char*)d_in[2]; a.w1 = (const char*)d_in[4]; a.b1 = (const char*)d_in[5];
    a.w2 = (const char*)d_in[6]; a.b2 = (const char*)d_in[7]; a.ws = (char*)d_ws; a.out = (char*)d_out;
    k_all<<<256, 1024, 0, stream>>>(a);
}
```

```cpp
#include <hip/hip_runtime.h>
#include <hip/hip_bf16.h>
#include <stdint.h>
#include <type_traits>
#include <stdio.h>

typedef __bf16 bf16x8 __attribute__((ext_vector_type(8)));
typedef __bf16 bf16x4 __attribute__((ext_vector_type(4)));
typedef __bf16 bf16x2 __attribute__((ext_vector_type(2)));
typedef float f32x16 __attribute__((ext_vector_type(16)));
typedef float f32x8 __attribute__((ext_vector_type(8)));
typedef float f32x4 __attribute__((ext_vector_type(4)));
typedef float f32x2 __attribute__((ext_vector_type(2)));
typedef short s16x4 __attribute__((ext_vector_type(4)));
typedef unsigned u32x4 __attribute__((ext_vector_type(4)));
typedef unsigned u32x2 __attribute__((ext_vector_type(2)));
#define LAS __attribute__((address_space(3)))
#define DI __device__ __forceinline__
typedef LAS char* lptr;
typedef __attribute__((address_space(1))) unsigned gu32;

constexpr int QT = 1280;
constexpr int KVT = 2688;
constexpr int NKS = 136;
constexpr size_t WS_FLAG = 0;
constexpr size_t WS_Q = 131072;
constexpr size_t WS_KV = WS_Q + (size_t)32 * 64 * QT;
constexpr size_t WS_AF = WS_KV + (size_t)32 * 64 * KVT;
constexpr size_t WS_YF = WS_AF + (size_t)64 * NKS * 1024;
constexpr int F_LN = 16;
constexpr int F_QKV = 32;
constexpr int F_H = F_QKV + 240;
constexpr int F_O = F_H + 256;
constexpr float QSCALE = 0.22360679774997896f * 1.4426950408889634f;

constexpr int KV_STAGE = 4 * KVT, KV_HALF = 3 * KV_STAGE;
constexpr int W2_BASE = 65536, W2_STAGE = 128 * 160;
constexpr int CTL = 155648, L_FST = CTL + 512, L_FCS = CTL + 1536, L_DUMMY = CTL + 2048, L_PST = CTL + 2560, L_PCS = L_PST + 4096;
enum { C_QFREE = 0, C_QFULL = 8, C_PART = 12, C_TAG = 20, C_TAGRDY, C_STAT, C_QDONE, C_KVGO, C_YRDY, C_HRDY, C_HDONE, C_ODONE, C_B12, C_LNDONE,
       C_KVFULL = 32  , C_KVFREE = 40, C_WFREE = 48, C_XCH = 52, C_WFULL = 56 };

#define MFMA32(a, b, c) __builtin_amdgcn_mfma_f32_32x32x16_bf16((a), (b), (c), 0, 0, 0)
#define MFMA16(a, b, c) __builtin_amdgcn_mfma_f32_16x16x32_bf16((a), (b), (c), 0, 0, 0)
#define MFMA8(a, b, c) __builtin_amdgcn_mfma_f32_32x32x8bf16_1k(__builtin_bit_cast(s16x4, a), __builtin_bit_cast(s16x4, b), (c), 0, 0, 0)
#define STAMP_DECL
#define STAMP(i)
#define STAMP_FLUSH(K)
#define SBAR() __builtin_amdgcn_sched_barrier(0)
#define CFENCE() asm volatile("" ::: "memory")
#define PIN(x) asm volatile("" : "+v"(x))
#define LBAR() asm volatile("s_waitcnt lgkmcnt(0)\n\ts_barrier" ::: "memory")
DI int lane_id() { unsigned z = 0; asm volatile("" : "+s"(z)); return __builtin_amdgcn_mbcnt_hi(~0u, __builtin_amdgcn_mbcnt_lo(~0u, z)); }
template <int N> DI void wait_vm() { asm volatile("s_waitcnt vmcnt(%0)" :: "i"(N) : "memory"); }
template <int I, int N, class F> DI void static_for(F&& f) { if constexpr (I < N) { f(std::integral_constant<int, I>{}); static_for<I + 1, N>(f); } }

template <int OFF, bool DEV>
DI void glds16(const void* sbase, unsigned voff, unsigned lds) {
    if constexpr (DEV) asm volatile("s_mov_b32 m0, %2\n\ts_nop 3\n\tglobal_load_lds_dwordx4 %0, %1 offset:%3 sc1" :: "v"(voff), "s"(sbase), "s"(lds), "i"(OFF) : "memory", "m0");
    else               asm volatile("s_mov_b32 m0, %2\n\ts_nop 3\n\tglobal_load_lds_dwordx4 %0, %1 offset:%3" :: "v"(voff), "s"(sbase), "s"(lds), "i"(OFF) : "memory", "m0");
}
DI void glds_4k(const void* sbase, unsigned voff, unsigned lds) {
    asm volatile("s_mov_b32 m0, %2\n\ts_nop 3\n\tglobal_load_lds_dwordx4 %0, %1 sc1\n\tglobal_load_lds_dwordx4 %0, %1 offset:1024 sc1\n\t"
                 "global_load_lds_dwordx4 %0, %1 offset:2048 sc1\n\tglobal_load_lds_dwordx4 %0, %1 offset:3072 sc1" :: "v"(voff), "s"(sbase), "s"(lds) : "memory", "m0");
}
DI void touch(const void* sbase, unsigned voff, unsigned lds) {
    asm volatile("s_mov_b32 m0, %2\n\ts_nop 3\n\tglobal_load_lds_dword %0, %1 sc1" :: "v"(voff), "s"(sbase), "s"(lds) : "memory", "m0");
}
template <int OFF> DI void gld128(u32x4& d, const void* sbase, unsigned voff) {
    asm volatile("global_load_dwordx4 %0, %1, %2 offset:%3" : "=v"(d) : "v"(voff), "s"(sbase), "i"(OFF) : "memory"); }
template <int OFF> DI void gld128_dev(u32x4& d, const void* sbase, unsigned voff) {
    asm volatile("global_load_dwordx4 %0, %1, %2 offset:%3 sc1" : "=v"(d) : "v"(voff), "s"(sbase), "i"(OFF) : "memory"); }
template <int OFF> DI void gld128_dev_into(u32x4& d, const void* sbase, unsigned voff) {
    asm volatile("global_load_dwordx4 %0, %1, %2 offset:%3 sc1" : "+v"(d) : "v"(voff), "s"(sbase), "i"(OFF) : "memory"); }
template <int OFF> DI void gld64(u32x2& d, const void* sbase, unsigned voff) {
    asm volatile("global_load_dwordx2 %0, %1, %2 offset:%3" : "=v"(d) : "v"(voff), "s"(sbase), "i"(OFF) : "memory"); }
template <int OFF> DI void gld64_dev(u32x2& d, const void* sbase, unsigned voff) {
    asm volatile("global_load_dwordx2 %0, %1, %2 offset:%3 sc1" : "=v"(d) : "v"(voff), "s"(sbase), "i"(OFF) : "memory"); }
DI void gst128_dev(void* sbase, unsigned voff, u32x4 v) {
    asm volatile("global_store_dwordx4 %0, %1, %2 sc1\n\ts_nop 1" :: "v"(voff), "v"(v), "s"(sbase) : "memory"); }
DI void gst64_dev(void* sbase, unsigned voff, u32x2 v) {
    asm volatile("global_store_dwordx2 %0, %1, %2 sc1\n\ts_nop 1" :: "v"(voff), "v"(v), "s"(sbase) : "memory"); }

DI LAS unsigned* cw(lptr lds, int i) { return (LAS unsigned*)(lds + CTL) + i; }
DI unsigned cw_get(lptr lds, int i) { return __hip_atomic_load(cw(lds, i), __ATOMIC_RELAXED, __HIP_MEMORY_SCOPE_WORKGROUP); }
template <int LANES = 1> DI void cw_set(lptr lds, int i, unsigned v) {
    asm volatile("s_mov_b64 exec, %2\n\tds_write_b32 %0, %1\n\ts_mov_b64 exec, -1" :: "v"((unsigned)(uintptr_t)cw(lds, i)), "v"(v), "i"((1 << LANES) - 1) : "memory");
}
DI unsigned cw_add(lptr lds, int i) {
    unsigned old = 0; CFENCE();
    if (lane_id() == 0) old = __hip_atomic_fetch_add(cw(lds, i), 1u, __ATOMIC_RELAXED, __HIP_MEMORY_SCOPE_WORKGROUP);
    CFENCE();
    return __builtin_amdgcn_readfirstlane(old);
}
template <int N> DI bool cw_ok(lptr lds, int i, unsigned need) { return __all(cw_get(lds, N > 1 ? i + (lane_id() & (N - 1)) : i) >= need); }
template <int N, int SLEEP> DI void cw_wait(lptr lds, int i, unsigned need) {
    const int k = N > 1 ? i + (lane_id() & (N - 1)) : i;
#pragma unroll 1
    for (unsigned spins = 0; spins < (1u << 18); ++spins) { if (__all(cw_get(lds, k) >= need)) break; __builtin_amdgcn_s_sleep(SLEEP); }
    CFENCE();
}
DI void flag_set(char* ws, int idx, unsigned tag) { if (lane_id() == 0) __hip_atomic_store((gu32*)(ws + WS_FLAG) + idx * 32, tag, __ATOMIC_RELAXED, __HIP_MEMORY_SCOPE_AGENT); }
template <int SLEEP> DI void flag_wait(char* ws, int idx  , unsigned tag) {
#pragma unroll 1
    for (unsigned spins = 0; spins < (1u << 17); ++spins) {
        if (__all(__hip_atomic_load((gu32*)(ws + WS_FLAG) + idx * 32, __ATOMIC_RELAXED, __HIP_MEMORY_SCOPE_AGENT) == tag)) break;
        __builtin_amdgcn_s_sleep(SLEEP);
    }
    CFENCE();
}
#define POLL_GAP 4
struct Polls { unsigned v0 = 0, v1 = 0, v2 = 0; DI void release() { asm volatile("" :: "v"(v0), "v"(v1), "v"(v2)); } };
DI void flag_wait_fast(Polls& pl, char* ws, int idx  , unsigned tag) {
    unsigned &v0 = pl.v0, &v1 = pl.v1, &v2 = pl.v2;
    const char* base = ws + WS_FLAG; const unsigned off = idx * 128;
#define POLL(v) asm volatile("global_load_dword %0, %1, %2 sc1" : "=v"(v) : "v"(off), "s"(base) : "memory")
#define SEEN(v) asm volatile("s_waitcnt vmcnt(2)" : "+v"(v) :: "memory"); if (__all(v == tag)) break; __builtin_amdgcn_s_sleep(POLL_GAP); POLL(v)
    wait_vm<0>();
    POLL(v0); __builtin_amdgcn_s_sleep(POLL_GAP); POLL(v1); __builtin_amdgcn_s_sleep(POLL_GAP); POLL(v2);
#pragma unroll 1
    for (unsigned spins = 0; spins < (1u << 16); ++spins) { SEEN(v0); SEEN(v1); SEEN(v2); }
    CFENCE();
#undef POLL
#undef SEEN
}
DI unsigned get_tag(lptr lds) { cw_wait<1, 2>(lds, C_TAGRDY, 1); return __builtin_amdgcn_readfirstlane(cw_get(lds, C_TAG)); }
DI void publish(lptr lds, int ctr, unsigned n, char* ws, int flag) {
    wait_vm<0>();
    if (cw_add(lds, ctr) == n - 1) flag_set(ws, flag, get_tag(lds));
}

template <int CTRL> DI float dpp(float x) { return __builtin_bit_cast(float, __builtin_amdgcn_mov_dpp(__builtin_bit_cast(int, x), CTRL, 0xf, 0xf, true)); }
DI float sum16(float v) { v += dpp<0xB1>(v); v += dpp<0x4E>(v); v += dpp<0x124>(v); v += dpp<0x128>(v); return v; }
DI f32x4 bf4_to_f32(u32x2 v) { return (f32x4){__uint_as_float(v.x << 16), __uint_as_float(v.x & 0xffff0000u), __uint_as_float(v.y << 16), __uint_as_float(v.y & 0xffff0000u)}; }
DI bf16x8 lds_b128(lptr p) { return *(const LAS bf16x8*)p; }
DI bf16x4 lds_b64(lptr p) { return *(const LAS bf16x4*)p; }
DI bf16x4 lds_tr(lptr p) { return __builtin_bit_cast(bf16x4, __builtin_amdgcn_ds_read_tr16_b64_v4i16((LAS s16x4*)p)); }
DI bf16x8 cat8(bf16x4 a, bf16x4 b) { return __builtin_shufflevector(a, b, 0, 1, 2, 3, 4, 5, 6, 7); }
DI f32x2 both_halves(float v) {
    auto r = __builtin_amdgcn_permlane32_swap(__float_as_uint(v), __float_as_uint(v), false, false);
    return (f32x2){__uint_as_float(r[0]), __uint_as_float(r[1])};
}
DI float max_halves(float v) { const f32x2 t = both_halves(v); return fmaxf(t.x, t.y); }
DI float opq(float v) { asm("" : "+v"(v)); return v; }
DI unsigned gelu2(float a, float b) {
    const f32x2 x = {a, b};
    const f32x2 z = x * __builtin_elementwise_fma(x * x, (f32x2){-0.044715f * 2.302208198f, -0.044715f * 2.302208198f}, (f32x2){-2.302208198f, -2.302208198f});
    const f32x2 d = (f32x2){__builtin_amdgcn_exp2f(z.x), __builtin_amdgcn_exp2f(z.y)} + 1.0f;
    const f32x2 y = x * (f32x2){__builtin_amdgcn_rcpf(d.x), __builtin_amdgcn_rcpf(d.y)};
    return __builtin_bit_cast(unsigned, __builtin_convertvector(y, bf16x2));
}

struct Args { const char *x, *wqkv, *wo, *w1, *b1, *w2, *b2; char *ws, *out; };

#define QLEAD 9
template <bool ISV>
DI void qkv_tile(int w, int rp, int ct, char* __restrict__ ws, lptr lds) {
    STAMP_DECL;
    STAMP(0);
    const int l = lane_id(), r = l & 31, hh = l >> 5, wr = w & 1, wc = w >> 1;
    const int g4 = l >> 4, q4 = (l & 15) >> 2, p4 = l & 3;
    unsigned aoff[4], boff[4];
#pragma unroll
    for (int i = 0; i < 4; ++i) {
        const int ks = (i + wc + 2 * wr) & 3;
        aoff[i] = (unsigned)((64 * wr + r) * 128 + (((2 * ks + hh) ^ ((r >> 1) & 7)) << 4));
        boff[i] = (unsigned)(16384 + ks * 4096 + (8 * (g4 >> 1) + q4) * 256 + (((4 * wc + 2 * (g4 & 1) + (p4 >> 1)) ^ (q4 << 2)) << 4) + (p4 & 1) * 8);
    }
    f32x16 acc[2] = {};
    f32x4 csum = {};
    const unsigned selw = ((l & 15) == (g4 & 1)) ? 0x3f803f80u : 0u;
    const bf16x8 sel = __builtin_bit_cast(bf16x8, (u32x4){selw, selw, selw, selw});
    float sx[2] = {0.f, 0.f}, sq[2] = {0.f, 0.f};
    bf16x8 a0[4], a1[4], b[4];
    auto rdb = [&](int h, int i) { b[i] = cat8(lds_tr(lds + boff[i] + h * 32768), lds_tr(lds + boff[i] + h * 32768 + 1024)); };
    auto rda = [&](int h, int i) { a0[i] = lds_b128(lds + aoff[i] + h * 32768); a1[i] = lds_b128(lds + aoff[i] + h * 32768 + 4096); };
    auto mm0 = [&](int i) {
 acc[0] = ISV ? MFMA32(a0[i], b[i], acc[0]) : MFMA32(b[i], a0[i], acc[0]); PIN(acc[0]); };
    auto mm1 = [&](int i) {
 acc[1] = ISV ? MFMA32(a1[i], b[i], acc[1]) : MFMA32(b[i], a1[i], acc[1]); PIN(acc[1]); };
    auto stats = [&](unsigned u, float& s, float& q) {
        const float lo = __uint_as_float(u << 16), hi = __uint_as_float(u & 0xffff0000u);
        s += lo; q = __builtin_fmaf(lo, lo, q); s += hi; q = __builtin_fmaf(hi, hi, q);
        PIN(s); PIN(q);
    };
    const int qword = (l & 8) ? C_QFULL + (l & 3) : C_QFREE + (l & 7);
    const unsigned qlead = (l & 8) ? 0 : QLEAD;
    cw_wait<4, 1>(lds, C_QFULL, 1);
    STAMP(1);
    rdb(0, 0); rda(0, 0); rdb(0, 1); rda(0, 1);
#pragma unroll 1
    for (int k2 = 0; k2 < 10; k2 += 2) {
#pragma unroll
        for (int h = 0; h < 2; ++h) {
            const u32x4 s0 = __builtin_bit_cast(u32x4, a0[0]), s1 = __builtin_bit_cast(u32x4, a1[0]);
            SBAR();
            mm0(0); rdb(h, 2); stats(s0[0], sx[0], sq[0]); SBAR();
            mm1(0); rda(h, 2); stats(s0[1], sx[0], sq[0]); SBAR();
            mm0(1); rdb(h, 3); stats(s0[2], sx[0], sq[0]); SBAR();
            mm1(1); rda(h, 3); cw_set(lds, C_QFREE + w, k2 + h + 1);
            const unsigned full = cw_get(lds, qword);
            stats(s0[3], sx[0], sq[0]); SBAR();
            csum = MFMA16(sel, b[0], csum); csum = MFMA16(sel, b[1], csum); PIN(csum);
            if (h == 1) {
#pragma unroll
                for (int i = 0; i < 4; ++i) { aoff[i] ^= 65536u; boff[i] ^= 65536u; }
            }
            SBAR();
            mm0(2); stats(s1[0], sx[1], sq[1]); SBAR();
            const unsigned need = k2 + h + 2 > 10 ? 10 : k2 + h + 2;
            if (!__all(full + qlead >= need)) {
#pragma unroll 1
                for (unsigned spins = 0; spins < (1u << 18); ++spins) { if (__all(cw_get(lds, qword) + qlead >= need)) break; __builtin_amdgcn_s_sleep(1); }
            }
            CFENCE();
            rdb(h ^ 1, 0); mm1(2); rda(h ^ 1, 0); stats(s1[1], sx[1], sq[1]); SBAR();
            mm0(3); rdb(h ^ 1, 1); stats(s1[2], sx[1], sq[1]); SBAR();
            mm1(3); rda(h ^ 1, 1); stats(s1[3], sx[1], sq[1]); SBAR();
        }
    }
#pragma unroll
    for (int mt = 0; mt < 2; ++mt) {
        const f32x2 ts = both_halves(sx[mt]), tq = both_halves(sq[mt]);
        *(LAS f32x2*)(lds + L_PST + (wc * 128 + 64 * wr + 32 * mt + r) * 8) = (f32x2){ts.x + opq(ts.y), tq.x + opq(tq.y)};
    }
    if (l < 16) { *(LAS float*)(lds + L_PCS + (wr * 128 + 32 * wc + l) * 4) = csum[0]; *(LAS float*)(lds + L_PCS + (wr * 128 + 32 * wc + 16 + l) * 4) = csum[1]; }
    cw_set(lds, C_PART + w, 1);
    STAMP(2);
    __builtin_amdgcn_s_setprio(3);
    cw_wait<1, 1>(lds, C_STAT, 3);
    STAMP(3);
    const LAS f32x2* fst = (const LAS f32x2*)(lds + L_FST);
    const LAS float* fcs = (const LAS float*)(lds + L_FCS);
    const int tt0 = rp * 4 + 2 * wr;
    if (ISV) {
        const int cc = (ct - 10) * 128 + 32 * wc + r, head = cc / 20, d = cc - head * 20;
        const float c = fcs[32 * wc + r];
        const unsigned off = (unsigned)(head * 64 * KVT + QT + (hh * 20 + d) * 16);
#pragma unroll
        for (int mt = 0; mt < 2; ++mt) {
            char* dst = ws + WS_KV + (size_t)(tt0 + mt) * KVT;
#pragma unroll
            for (int s = 0; s < 2; ++s) {
                f32x8 v;
#pragma unroll
                for (int j = 0; j < 8; ++j) {
                    const f32x2 ab = fst[64 * wr + 32 * mt + 16 * s + 8 * (j >> 2) + 4 * hh + (j & 3)];
                    v[j] = __builtin_fmaf(ab.x, acc[mt][8 * s + j], opq(ab.y) * c);
                }
                gst128_dev(dst + s * 640, off, __builtin_bit_cast(u32x4, __builtin_convertvector(v, bf16x8)));
            }
            if (d == 19) { const unsigned o_ = hh ? 0u : 0x3f803f80u; gst128_dev(dst, off + 1280 - 304 - hh * 304, (u32x4){o_, o_, o_, o_}); }
        }
    } else {
        const bool isq = ct < 5;
        const int tile = isq ? QT : KVT;
        char* dst = ws + (isq ? WS_Q : WS_KV) + (size_t)tt0 * tile;
        f32x2 ab[2]; float aby[2];
#pragma unroll
        for (int mt = 0; mt < 2; ++mt) { ab[mt] = fst[64 * wr + 32 * mt + r]; aby[mt] = opq(ab[mt].y); }
#pragma unroll
        for (int g = 0; g < 4; ++g) {
            const f32x4 c4 = *(const LAS f32x4*)(fcs + 32 * wc + 8 * g + 4 * hh);
            u32x2 pk[2];
#pragma unroll
            for (int mt = 0; mt < 2; ++mt) {
                f32x4 v;
#pragma unroll
                for (int j = 0; j < 4; ++j) v[j] = __builtin_fmaf(ab[mt].x, acc[mt][4 * g + j], aby[mt] * c4[j]);
                pk[mt] = __builtin_bit_cast(u32x2, __builtin_convertvector(v, bf16x4));
            }
            const int pi = (isq ? ct : ct - 5) * 16 + 4 * wc + g, k2 = pi / 5, e = pi - 5 * k2;
            if (e == 2) {
                char* p = dst + (size_t)(2 * k2) * 64 * tile + 1024;
                gst64_dev(p, hh * 64 * tile + r * 8, pk[0]); gst64_dev(p + tile, hh * 64 * tile + r * 8, pk[1]);
            } else {
                const auto sx = __builtin_amdgcn_permlane32_swap(pk[0].x, pk[1].x, false, false), sy = __builtin_amdgcn_permlane32_swap(pk[0].y, pk[1].y, false, false);
                gst128_dev(dst + (size_t)(2 * k2 + (e > 2)) * 64 * tile + (e > 2 ? e - 3 : e) * 512, hh * tile + r * 16, (u32x4){sx[0], sy[0], sx[1], sy[1]});
            }
        }
    }
    STAMP(4);
    publish(lds, C_QDONE, 8, ws, F_QKV + ct * 16 + rp);
    STAMP(5); STAMP_FLUSH(0);
}

DI void qkv_stats(int mw, bool isq, lptr lds) {
    const int i = (mw & 1) * 64 + lane_id();
    cw_wait<8, 8>(lds, C_QFREE, 9);
    cw_wait<8, 1>(lds, C_PART, 1);
    if (mw < 2) {
        const LAS f32x2* p = (const LAS f32x2*)(lds + L_PST) + i;
        const f32x2 t = (p[0] + p[128]) + (p[256] + p[384]);
        const float mean = t.x * (1.0f / 640.0f);
        const float var = fmaxf(opq(t.y) * (1.0f / 640.0f) - mean * mean, 0.f);
        const float a = __builtin_amdgcn_rsqf(var + 1e-5f) * (isq ? QSCALE : 1.0f);
        *(LAS f32x2*)(lds + L_FST + i * 8) = (f32x2){a, -a * mean};
    } else {
        const LAS f32x2* p = (const LAS f32x2*)(lds + L_PCS) + (i & 63);
        *(LAS f32x2*)(lds + L_FCS + (i & 63) * 8) = p[0] + p[64];
    }
    cw_add(lds, C_STAT);
}

DI void ln_rows(int w, int rp, const char* __restrict__ x, char* __restrict__ ws, lptr lds) {
    const int l = lane_id(), c0 = l & 15, row0 = rp * 128 + 8 * w;
    u32x4 raw[2][5];
#pragma unroll
    for (int i = 0; i < 2; ++i)
#pragma unroll
        for (int j = 0; j < 5; ++j) raw[i][j] = *(const u32x4*)(x + (size_t)(row0 + 4 * i + (l >> 4)) * 1280 + (c0 + 16 * j) * 16);
    SBAR();
#pragma unroll
    for (int i = 0; i < 2; ++i) {
        f32x2 v[20], s = {}, q = {};
#pragma unroll
        for (int j = 0; j < 5; ++j)
#pragma unroll
            for (int e = 0; e < 4; ++e) {
                const f32x2 t = {__uint_as_float(raw[i][j][e] << 16), __uint_as_float(raw[i][j][e] & 0xffff0000u)};
                v[4 * j + e] = t; s += t; q = __builtin_elementwise_fma(t, t, q);
            }
        const float mean = sum16(s.x + s.y) * (1.0f / 640.0f);
        const float var = fmaxf(sum16(q.x + q.y) * (1.0f / 640.0f) - mean * mean, 0.f);
        const float rs = __builtin_amdgcn_rsqf(var + 1e-5f);
        const float a = rs * __builtin_amdgcn_rsqf(var * rs * rs + 1e-5f), nb = -a * mean;
#pragma unroll
        for (int j = 0; j < 5; ++j)
#pragma unroll
            for (int e = 0; e < 4; ++e)
                raw[i][j][e] = __builtin_bit_cast(unsigned, __builtin_convertvector(__builtin_elementwise_fma(v[4 * j + e], (f32x2){a, a}, (f32x2){nb, nb}), bf16x2));
        SBAR();
    }
    char* dst = ws + WS_YF + (size_t)(row0 >> 5) * 40960 + (row0 & 31) * 16;
    lptr stg = lds + w * 6912;
#pragma unroll
    for (int part = 0; part < 2; ++part) {
#pragma unroll
        for (int j = 3 * part; j < (part ? 5 : 3); ++j)
#pragma unroll
            for (int i = 0; i < 2; ++i) *(LAS u32x4*)(stg + (c0 + 16 * (j - 3 * part)) * 144 + (4 * i + (l >> 4)) * 16) = raw[i][j];
#pragma unroll
        for (int j = 0; j < (part ? 4 : 6); ++j)
            gst128_dev(dst + (6 * part + j) * 4096, (l >> 3) * 512 + (l & 7) * 16, *(const LAS u32x4*)(stg + (8 * j + (l >> 3)) * 144 + (l & 7) * 16));
    }
}

DI float hmax16(const f32x16& s) {
    float a = fmaxf(fmaxf(s[0], s[1]), s[2]), b = fmaxf(fmaxf(s[3], s[4]), s[5]);
    a = fmaxf(fmaxf(a, s[6]), s[7]); b = fmaxf(fmaxf(b, s[8]), s[9]);
    a = fmaxf(fmaxf(a, s[10]), s[11]); b = fmaxf(fmaxf(b, s[12]), s[13]);
    return fmaxf(fmaxf(a, b), fmaxf(s[14], s[15]));
}
DI void set_ref(bf16x4& q2, float m, int hh) {
    const float nm = -m;
    const __bf16 a = (__bf16)nm; const float r1 = nm - (float)a;
    const __bf16 b = (__bf16)r1; const __bf16 c = (__bf16)(r1 - (float)b);
    if (hh) q2 = (bf16x4){a, b, c, (__bf16)0.f};
}

DI void attention(int w, int head, int qb, char* __restrict__ ws, lptr lds) {
    STAMP_DECL;
    const int g = w & 3, kh = w >> 2;
    const char* kvh = ws + WS_KV + (size_t)head * 64 * KVT;
    const f32x16 zero = {};
    bf16x8 q1[2]; bf16x4 q2[2];
    f32x16 o[2] = {};
    {
        const int l = lane_id(), r = l & 31, hh = l >> 5;
        const unsigned k2rel = hh ? 2560 : 1024 + r * 8;
        cw_wait<1, 2>(lds, C_KVGO, 1);
        STAMP(0);
        u32x4 q1r[2], k1r; u32x2 q2r[2], k2r;
        const char* qp = ws + WS_Q + (size_t)(head * 64 + qb * 8 + 2 * g) * QT;
        gld128_dev<0>(k1r, kvh + qb * KV_STAGE, l * 16); gld64_dev<0>(k2r, kvh + qb * KV_STAGE, k2rel);
        gld128_dev<0>(q1r[0], qp, l * 16); gld128_dev<QT>(q1r[1], qp, l * 16);
        gld64_dev<1024>(q2r[0], qp, r * 8); gld64_dev<1024 + QT>(q2r[1], qp, r * 8);
        wait_vm<0>();
        PIN(q1r[0]); PIN(q1r[1]); PIN(q2r[0]); PIN(q2r[1]); PIN(k1r); PIN(k2r);
#pragma unroll
        for (int i = 0; i < 2; ++i) {
            q1[i] = __builtin_bit_cast(bf16x8, q1r[i]);
            q2[i] = hh ? (bf16x4){} : __builtin_bit_cast(bf16x4, q2r[i]);
            f32x16 s = MFMA8(k2r, q2[i], zero); s = MFMA32(__builtin_bit_cast(bf16x8, k1r), q1[i], s);
            set_ref(q2[i], max_halves(hmax16(s)), hh);
        }
        const unsigned k1off = (unsigned)(kh * KV_HALF + l * 16);
        const unsigned k2off = (unsigned)(kh * KV_HALF) + k2rel;
        const unsigned v0off = (unsigned)(kh * KV_HALF + (r < 20 ? QT + (hh * 20 + r) * 16 : (r == 20 ? 2560 : 2576)));
        const unsigned v1off = v0off + (r < 20 ? 640 : 0);
        cw_wait<1, 1>(lds, C_KVFULL + w, 1);
        STAMP(1);
        if (kh) __builtin_amdgcn_s_setprio(0); else __builtin_amdgcn_s_setprio(1);
        f32x16 sa, sb;
        bf16x8 k1 = lds_b128(lds + k1off); bf16x4 k2 = lds_b64(lds + k2off);
        bf16x8 v0 = lds_b128(lds + v0off), v1 = lds_b128(lds + v1off);
        bf16x8 p1 = {};
        float ta = 0.f, tb = 0.f;
        sa = MFMA8(k2, q2[0], zero); sa = MFMA32(k1, q1[0], sa);
        sb = MFMA8(k2, q2[1], zero);
#define EX(x) __builtin_amdgcn_exp2f(x)
#define CV(P, i, a, b) do { const f32x2 t_ = {a, b}; const bf16x2 c_ = __builtin_convertvector(t_, bf16x2); P[2 * (i)] = c_[0]; P[2 * (i) + 1] = c_[1]; } while (0)
#define GRP(SC, j, PA, ia, PB, ib) do { float e0_ = EX(SC[j]), e1_ = EX(SC[j + 1]); PIN(e0_); PIN(e1_); CV(PA, ia, ta, tb); PIN(PA); SBAR(); \
        ta = EX(SC[j + 2]); tb = EX(SC[j + 3]); PIN(ta); PIN(tb); CV(PB, ib, e0_, e1_); PIN(PB); SBAR(); } while (0)
#define UNIT(SC, SN, IC, IN, AFTER_D, AFTER_B, AFTER_C) do { bf16x8 p0; \
        GRP(SC, 0, p1, 3, p0, 0);  o[IN] = MFMA32(v1, p1, o[IN]); PIN(o[IN]); AFTER_D; SBAR(); \
        GRP(SC, 4, p0, 1, p0, 2);  SN = MFMA32(k1, q1[IN], SN); PIN(SN); AFTER_B; SBAR(); \
        GRP(SC, 8, p0, 3, p1, 0);  o[IC] = MFMA32(v0, p0, o[IC]); PIN(o[IC]); AFTER_C; SBAR(); \
        GRP(SC, 12, p1, 1, p1, 2); SC = MFMA8(k2, q2[IC], zero); PIN(SC); SBAR(); \
        } while (0)
        int buf = 0;
        for (int s = 0; s < 8; ++s) {
            const int nbuf = buf == 2 ? 0 : buf + 1;
            lptr cb = lds + buf * KV_STAGE;
            lptr nb = lds + nbuf * KV_STAGE;
            unsigned full = 0;
#pragma unroll
            for (int j = 0; j < 4; ++j) {
                lptr cur = cb + j * KVT;
                lptr nxt = j == 3 ? nb : cb + (j + 1) * KVT;
#define REL() do { if (j == 3) { cw_set(lds, C_KVFREE + w, s + 1); full = cw_get(lds, C_KVFULL + w); } } while (0)
#define ACQ() do { if (j == 3) { if (__builtin_amdgcn_readfirstlane(full) < (unsigned)(s + 2) && s < 7) cw_wait<1, 1>(lds, C_KVFULL + w, s + 2); CFENCE(); } } while (0)
                UNIT(sa, sb, 0, 1, v1 = lds_b128(cur + v1off); REL(), ACQ(); k2 = lds_b64(nxt + k2off); k1 = lds_b128(nxt + k1off), (void)0);
                UNIT(sb, sa, 1, 0, (void)0, (void)0, v0 = lds_b128(nxt + v0off));
#undef REL
#undef ACQ
            }
            buf = nbuf;
        }
        CV(p1, 3, ta, tb);
        o[1] = MFMA32(v1, p1, o[1]);
#undef UNIT
#undef GRP
    }
    STAMP(2);
    __builtin_amdgcn_s_setprio(3);
    const int l = lane_id(), r = l & 31, hh = l >> 5;
    if (kh == 0) {
        cw_wait<4, 1>(lds, C_KVFREE, 8);
#pragma unroll
        for (int i = 0; i < 2; ++i)
#pragma unroll
            for (int q = 0; q < 3; ++q)
                *(LAS f32x4*)(lds + ((g * 2 + i) * 3 + q) * 1024 + l * 16) = (f32x4){o[i][4 * q], o[i][4 * q + 1], o[i][4 * q + 2], o[i][4 * q + 3]};
        cw_set(lds, C_XCH + g, 1);
        STAMP(3); STAMP_FLUSH(1);
        return;
    }
    cw_wait<1, 1>(lds, C_XCH + g, 1);
#pragma unroll
    for (int i = 0; i < 2; ++i) {
        f32x4 res[3];
#pragma unroll
        for (int q = 0; q < 3; ++q)
            res[q] = *(const LAS f32x4*)(lds + ((g * 2 + i) * 3 + q) * 1024 + l * 16) + (f32x4){o[i][4 * q], o[i][4 * q + 1], o[i][4 * q + 2], o[i][4 * q + 3]};
        float chk = 0.f;
#pragma unroll
        for (int q = 0; q < 3; ++q)
#pragma unroll
            for (int e = 0; e < 4; ++e) chk += fabsf(res[q][e]);
        if (__any(!(chk < 1.0e37f))) {
            const unsigned k2rel = hh ? 2560 : 1024 + r * 8;
            const unsigned vrel = r < 20 ? QT + (hh * 20 + r) * 16 : (r == 20 ? 2560 : 2576);
            bf16x4 qz = q2[i]; set_ref(qz, 0.f, hh);
            float m = -INFINITY;
#pragma unroll 1
            for (int t = 0; t < 64; ++t) {
                u32x4 a1; u32x2 a2;
                gld128_dev<0>(a1, kvh + t * KVT, l * 16); gld64_dev<0>(a2, kvh + t * KVT, k2rel);
                wait_vm<0>(); PIN(a1); PIN(a2);
                f32x16 s = MFMA8(a2, qz, zero); s = MFMA32(__builtin_bit_cast(bf16x8, a1), q1[i], s); m = fmaxf(m, hmax16(s));
            }
            set_ref(qz, max_halves(m), hh);
            f32x16 of = {};
#pragma unroll 1
            for (int t = 0; t < 64; ++t) {
                u32x4 a1, b0, b1; u32x2 a2;
                gld128_dev<0>(a1, kvh + t * KVT, l * 16); gld64_dev<0>(a2, kvh + t * KVT, k2rel);
                gld128_dev<0>(b0, kvh + t * KVT, vrel); gld128_dev<0>(b1, kvh + t * KVT, vrel + (r < 20 ? 640 : 0));
                wait_vm<0>(); PIN(a1); PIN(a2); PIN(b0); PIN(b1);
                f32x16 s = MFMA8(a2, qz, zero); s = MFMA32(__builtin_bit_cast(bf16x8, a1), q1[i], s);
                bf16x8 pa, pb;
#pragma unroll
                for (int e = 0; e < 4; ++e) { CV(pa, e, EX(s[2 * e]), EX(s[2 * e + 1])); CV(pb, e, EX(s[8 + 2 * e]), EX(s[9 + 2 * e])); }
                of = MFMA32(__builtin_bit_cast(bf16x8, b0), pa, of); of = MFMA32(__builtin_bit_cast(bf16x8, b1), pb, of);
            }
#pragma unroll
            for (int q = 0; q < 3; ++q) res[q] = (f32x4){of[4 * q], of[4 * q + 1], of[4 * q + 2], of[4 * q + 3]};
        }
        const float inv = __builtin_amdgcn_rcpf(opq(both_halves(res[2][0]).y));
#pragma unroll
        for (int q = 0; q < 3; ++q) res[q] *= inv;
        char* af = ws + WS_AF + ((size_t)(qb * 8 + 2 * g + i) * NKS + 96) * 1024;
        gst128_dev(af + head * 1024, l * 16, __builtin_bit_cast(u32x4, cat8(__builtin_convertvector(res[0], bf16x4), __builtin_convertvector(res[1], bf16x4))));
        if (!hh) gst64_dev(af + (32 + (head >> 2)) * 1024 + ((head >> 1) & 1) * 512 + (head & 1) * 8, r * 16, __builtin_bit_cast(u32x2, __builtin_convertvector(res[2], bf16x4)));
    }
#undef CV
#undef EX
    publish(lds, C_ODONE, 4, ws, F_O + qb * 32 + head);
    STAMP(3); STAMP_FLUSH(1);
}

#define HSLEEP 1
#define PACE 5
#define NOPS(n) do { SBAR(); asm volatile("s_nop %0" :: "i"(n)); SBAR(); } while (0)
DI int w1_slot(int st) { return (st == 0 || st == 3) ? 131072 : (st == 2 ? 98304 : 65536); }

__global__ __launch_bounds__(1024, 4) void k_all(Args a) {
    __shared__ __attribute__((aligned(16))) char smem[163840];
    lptr lds = (lptr)smem;
    char* __restrict__ ws = a.ws;
    const int b = blockIdx.x, xcd = b & 7, bi = b >> 3;
    const int w = __builtin_amdgcn_readfirstlane(threadIdx.x >> 6);
    const int rp = (xcd >> 1) * 4 + (bi < 28 ? (bi & 3) : 2 * (xcd & 1) + (bi & 1)), ct = bi < 28 ? 8 * (xcd & 1) + (bi >> 2) : (bi < 30 ? 7 : 15);
    const int head = xcd + 8 * (bi & 3), qb = bi >> 2;
    if (w == 0) *cw(lds, lane_id()) = 0u;
    LBAR();
    if (w == 11) {
        cw_set(lds, C_TAG, __hip_atomic_load((gu32*)(ws + WS_FLAG), __ATOMIC_RELAXED, __HIP_MEMORY_SCOPE_AGENT) + 1u);
        cw_set(lds, C_TAGRDY, 1);
    }
    if (ct == 15) {
        ln_rows(w, rp, a.x, ws, lds);
        wait_vm<0>();
        if (cw_add(lds, C_LNDONE) == 15) {
            flag_set(ws, F_LN + rp, get_tag(lds));
            cw_set<4>(lds, C_QFREE + (lane_id() & 3), 10); cw_set<4>(lds, C_QFREE + 4 + (lane_id() & 3), 10); cw_set(lds, C_QDONE, 8);
        }
    }
    if (w < 8) {
        __builtin_amdgcn_s_setprio(2);
        if (ct < 10) qkv_tile<false>(w, rp, ct, ws, lds);
        else if (ct < 15) qkv_tile<true>(w, rp, ct, ws, lds);
        int wa = w; asm volatile("" : "+s"(wa));
        attention(wa, head, qb, ws, lds);
        return;
    }
    STAMP_DECL;
    STAMP(0);
    const int panel = 4 * (xcd >> 1) + (bi & 3), hct = 8 * (xcd & 1) + (bi >> 2);
    const int rb = 4 * xcd + (bi & 3), nt = bi >> 2;
    const unsigned lds0 = (unsigned)(uintptr_t)lds;
    const int l = lane_id(), g4 = l >> 4, q4 = (l & 15) >> 2, p4 = l & 3, r16 = l & 15;
    const unsigned l16 = l * 16;
    const int w8 = w - 8, strip = w8 & 1, kq = w8 >> 1;
    const unsigned aoff = (unsigned)(g4 * 512 + r16 * 16);
    const unsigned b2off = (unsigned)(W2_BASE + (16 * (g4 >> 1) + 4 * (g4 & 1) + q4) * 160 + p4 * 8);
    f32x4 acc[2][5];
    unsigned vw[5];
    auto w2_offsets = [&](int kind) {
#pragma unroll
        for (int i = 0; i < 5; ++i) {
            const int c = 64 * i + l, rho = c / 10, ch = c - rho * 10, e = (rho >> 2) & 3;
            const int row = kind == 0 ? rho : (kind == 1 ? rho + (rho >> 4) * 4 : (4 * (rho >> 4) + 2 * (e & 1) + (e >> 1)) * 20 + 16 + (rho & 3));
            vw[i] = (unsigned)(row * 1280 + ch * 16);
        }
    };
    auto stb = [&](int c) {
        const int j = w8 & 1;
        const unsigned dst = lds0 + (unsigned)(W2_BASE + (c & 3) * W2_STAGE + j * 10240);
        const int gs = c < 12 ? 32 : (c < 16 ? 40 : 160);
        const char* p = (c < 12 ? a.w2 + (size_t)c * 128 * 1280 : a.wo + (size_t)(c < 16 ? (c - 12) * 160 : 0) * 1280) + (size_t)(2 * j * gs) * 1280 + nt * 160;
#pragma unroll
        for (int h = 0; h < 2; ++h)
#pragma unroll
            for (int i = 0; i < 5; ++i) glds16<0, false>(p + (size_t)h * gs * 1280, vw[i], dst + h * 5120 + i * 1024);
    };
    const unsigned boff = b2off + (unsigned)(kq * 5120);
    bf16x8 b12[5];
    auto load_b12 = [&]() {
        cw_wait<2, 8>(lds, C_WFULL, 18);
#pragma unroll
        for (int n = 0; n < 5; ++n) { b12[n] = cat8(lds_tr(lds + boff + n * 32), lds_tr(lds + boff + n * 32 + 1280)); PIN(b12[n]); }
        cw_add(lds, C_B12);
    };
    if (w8 < 4) {
        __builtin_amdgcn_s_setprio(3);
        if (w8 == 2) {
            cw_wait<8, 8>(lds, C_QFREE, 5);
            flag_wait<16>(ws, F_LN + 4 * (xcd >> 1) + (l & 3), get_tag(lds));
            const char* p = ws + WS_YF + (size_t)(xcd >> 1) * 655360 + bi * 20480;
            touch(p, l * 128, lds0 + L_DUMMY); touch(p + 8192, l * 128, lds0 + L_DUMMY); if (l < 32) touch(p + 16384, l * 128, lds0 + L_DUMMY);
            cw_set(lds, C_YRDY, 1);
        }
        if (ct < 15 && w8 != 2) qkv_stats(w8, ct < 5, lds);
        __builtin_amdgcn_s_setprio(2);
        {
            cw_wait<1, 4>(lds, C_YRDY, 1);
            const char* ysrc = ws + WS_YF + (size_t)(panel * 4 + w8) * 40960;
            unsigned bias[3];
#pragma unroll
            for (int t = 0; t < 3; ++t) asm volatile("global_load_ushort %0, %1, %2 offset:%3" : "=v"(bias[t]) : "v"((l & 31) * 2), "s"(a.b1 + hct * 192), "i"(t * 64) : "memory");
            u32x4 ya[8];
            static_for<0, 8>([&](auto I) { constexpr int i = I; gld128_dev<(i & 3) * 1024>(ya[i], ysrc + (i >> 2) * 4096, l16); });
            const unsigned boff = (unsigned)((8 * (g4 >> 1) + q4) * 192 + (g4 & 1) * 32 + p4 * 8);
            f32x16 hc[3];
            wait_vm<8>();
            const u32x2 one0 = {l < 32 ? 0x3f80u : 0u, 0u};
#pragma unroll
            for (int t = 0; t < 3; ++t) { PIN(bias[t]); hc[t] = MFMA8(((u32x2){bias[t], 0u}), one0, ((f32x16){})); }
#pragma unroll 1
            for (int st = 0; st < 5; ++st) {
                cw_wait<2, 2>(lds, C_WFULL, st + 1);
                if (st == 0) { STAMP(1); }
                lptr sb = lds + w1_slot(st) + boff;
                const char* ynext = ysrc + (st < 4 ? st + 1 : 4) * 8192;
                static_for<0, 8>([&](auto I) {
                    constexpr int i = I;
                    bf16x8 bw[3];
#pragma unroll
                    for (int t = 0; t < 3; ++t) bw[t] = cat8(lds_tr(sb + i * 3072 + t * 64), lds_tr(sb + i * 3072 + t * 64 + 768));
                    if (i == 7) cw_set(lds, C_WFREE + w8, st + 1);
                    wait_vm<7>();
                    PIN(ya[i]);
#pragma unroll
                    for (int t = 0; t < 3; ++t) { hc[t] = MFMA32(bw[t], __builtin_bit_cast(bf16x8, ya[i]), hc[t]); PIN(hc[t]); }
                    SBAR();
                    gld128_dev_into<(i & 3) * 1024>(ya[i], ynext + (i >> 2) * 4096, l16);
                });
            }
            wait_vm<0>();
#pragma unroll
            for (int i = 0; i < 8; ++i) asm volatile("" :: "v"(ya[i]));
            STAMP(2);
            char* hdst = ws + WS_AF + ((size_t)(panel * 4 + w8) * NKS + hct * 6) * 1024;
#pragma unroll
            for (int t = 0; t < 3; ++t)
#pragma unroll
                for (int s = 0; s < 2; ++s) {
                    u32x4 v;
#pragma unroll
                    for (int e = 0; e < 4; ++e) v[e] = gelu2(hc[t][8 * s + 2 * e], hc[t][8 * s + 2 * e + 1]);
                    gst128_dev(hdst + (2 * t + s) * 1024, l16, v);
                }
            publish(lds, C_HDONE, 4, ws, F_H + panel * 16 + hct);
            STAMP(3);
        }
        const char* asrc = ws + WS_AF + ((size_t)(2 * rb + strip) * NKS + 4 * kq) * 1024;
        u32x4 an[3][4];
        cw_wait<1, 8>(lds, C_HRDY, 1);
        STAMP(4);
#pragma unroll
        for (int u = 0; u < 3; ++u) {
            const char* p = asrc + u * 8192;
            gld128_dev<0>(an[u][0], p, aoff); gld128_dev<256>(an[u][1], p, aoff); gld128_dev<2048>(an[u][2], p, aoff); gld128_dev<2304>(an[u][3], p, aoff);
        }
#pragma unroll
        for (int ti = 0; ti < 10; ++ti) acc[ti / 5][ti % 5] = (f32x4){};
#pragma unroll 1
        for (int c3 = 0; c3 < 12; c3 += 3) {
#pragma unroll
            for (int u = 0; u < 3; ++u) {
                const int c = c3 + u;
                cw_wait<2, 4>(lds, C_WFULL, 6 + c);
                wait_vm<8>();
#pragma unroll
                for (int i = 0; i < 4; ++i) PIN(an[u][i]);
                lptr sb = lds + (c & 3) * W2_STAGE + kq * (64 * 160) + b2off;
#pragma unroll
                for (int i = 0; i < 2; ++i) {
                    bf16x8 bw[5];
#pragma unroll
                    for (int n = 0; n < 5; ++n) bw[n] = cat8(lds_tr(sb + i * 5120 + n * 32), lds_tr(sb + i * 5120 + n * 32 + 1280));
                    if (i == 1) cw_set(lds, C_WFREE + w8, 6 + c);
#pragma unroll
                    for (int n = 0; n < 5; ++n) {
                        acc[0][n] = MFMA16(bw[n], __builtin_bit_cast(bf16x8, an[u][2 * i]), acc[0][n]); PIN(acc[0][n]); NOPS(PACE);
                        acc[1][n] = MFMA16(bw[n], __builtin_bit_cast(bf16x8, an[u][2 * i + 1]), acc[1][n]); PIN(acc[1][n]); NOPS(PACE);
                    }
                }
                SBAR();
                const char* p = asrc + (c + 3 < 12 ? c + 3 : 11) * 8192;
                gld128_dev_into<0>(an[u][0], p, aoff); gld128_dev_into<256>(an[u][1], p, aoff); gld128_dev_into<2048>(an[u][2], p, aoff); gld128_dev_into<2304>(an[u][3], p, aoff);
            }
        }
        wait_vm<0>();
#pragma unroll
        for (int i = 0; i < 12; ++i) asm volatile("" :: "v"(an[i >> 2][i & 3]));
    } else {
        const int hw = w8 - 4;
        __builtin_amdgcn_s_setprio(3);
        auto w1_touch = [&]() {
            const int ln = l < 60 ? l : 0, row = ln / 12;
            touch(a.w1 + (size_t)((bi * 4 + hw) * 5) * 3072 + (xcd & 1) * 1536, row * 3072 + (ln - row * 12) * 128, lds0 + L_DUMMY);
        };
        if (ct < 15) {
            const char* xa = a.x + (size_t)rp * 128 * 1280;
            const char* wb = a.wqkv + ct * 256;
            unsigned va[4], vb[4];
#pragma unroll
            for (int i = 0; i < 4; ++i) {
                va[i] = (unsigned)((32 * hw + 8 * i + (l >> 3)) * 1280 + (((l & 7) ^ (4 * (i & 1) + (l >> 4))) << 4));
                vb[i] = (unsigned)((16 * hw + 4 * i + (l >> 4)) * 3840 + (((l & 15) ^ ((l >> 4) << 2)) << 4));
            }
            auto stage = [&](int kt) {
                const unsigned dst = lds0 + (unsigned)hw * 4096u + (unsigned)(kt & 3) * 32768u;
                const char* pa = xa + kt * 128;
                const char* pb = wb + (size_t)kt * 64 * 3840;
#pragma unroll
                for (int i = 0; i < 4; ++i) { glds16<0, false>(pa, va[i], dst + i * 1024); glds16<0, false>(pb, vb[i], dst + 16384 + i * 1024); }
            };
#pragma unroll 1
            for (int kt = 0; kt < 10; ++kt) {
                if (kt >= 3) {
                    wait_vm<16>(); cw_set(lds, C_QFULL + hw, kt - 2);
                    if (!cw_ok<8>(lds, C_QFREE, kt - 3)) {
                        wait_vm<8>(); cw_set(lds, C_QFULL + hw, kt - 1);
                        cw_wait<8, HSLEEP>(lds, C_QFREE, kt - 3);
                    }
                    CFENCE();
                }
                stage(kt);
            }
        } else cw_wait<8, 8>(lds, C_QFREE, 10);
        w1_touch();
        wait_vm<17>(); cw_set(lds, C_QFULL + hw, 8);
        wait_vm<9>(); cw_set(lds, C_QFULL + hw, 9);
        wait_vm<1>(); cw_set(lds, C_QFULL + hw, 10);
        STAMP(1);
        const unsigned tag = get_tag(lds);
        if (hw < 2) {
            const int c0 = head * 20, ta = c0 >> 7, tb = (c0 + 19) >> 7;
            const int kvf = F_QKV + (5 + 5 * (l & 1) + ((l & 2) ? tb : ta)) * 16 + 8 * hw;
            const char* src = ws + WS_KV + (size_t)(head * 64 + hw * 32) * KVT;
            Polls pl;
            if (hw == 1) cw_wait<1, 1>(lds, C_KVGO, 1);
            else {
                cw_wait<1, 2>(lds, C_QDONE, 8);
                flag_wait_fast(pl, ws, (l & 8) ? F_QKV + ((l & 2) ? tb : ta) * 16 + 2 * qb + (l & 1) : kvf + 2 * (l & 4) + qb, tag);
                cw_set(lds, C_KVGO, 1);
            }
            STAMP(2);
            int buf = 0;
#pragma unroll 1
            for (int s = 0; s < 8; ++s) {
                if (s == 1) flag_wait<2>(ws, kvf + ((l >> 2) & 7), tag);
                if (s >= 3) cw_wait<4, 8>(lds, C_KVFREE + 4 * hw, s - 2);
                const unsigned dst = lds0 + (unsigned)(hw * KV_HALF + buf * KV_STAGE);
                const char* p = src + ((s + qb) & 7) * KV_STAGE;
                glds_4k(p, l16, dst); glds_4k(p + 4096, l16, dst + 4096);
                glds16<0, true>(p + 8192, l16, dst + 8192); glds16<1024, true>(p + 8192, l16, dst + 8192);
                if (l < 32) glds16<2048, true>(p + 8192, l16, dst + 8192);
                wait_vm<0>(); cw_set<4>(lds, C_KVFULL + 4 * hw + (l & 3), s + 1);
                buf = buf == 2 ? 0 : buf + 1;
                if (s == 2) {
                    const int u = bi * 2 + hw;
                    touch(a.w2 + u * 30720, l * 128, lds0 + L_DUMMY); touch(a.w2 + u * 30720 + 8192, l * 128, lds0 + L_DUMMY); touch(a.w2 + u * 30720 + 16384, l * 128, lds0 + L_DUMMY);
                    if (l < 48) touch(a.w2 + u * 30720 + 24576, l * 128, lds0 + L_DUMMY);
                    touch(a.wo + u * 12800, l * 128, lds0 + L_DUMMY); if (l < 36) touch(a.wo + u * 12800 + 8192, l * 128, lds0 + L_DUMMY);
                }
            }
            pl.release();
            if (b == 0 && hw == 0) {
#pragma unroll 1
                for (int i = 0; i < 4; ++i) flag_wait<16>(ws, F_H + 64 * i + l, tag);
                flag_set(ws, 0, tag);
            }
        } else {
            const int j = hw - 2;
            __builtin_amdgcn_s_setprio(2);
            unsigned vo[3];
#pragma unroll
            for (int i = 0; i < 3; ++i) { const int c = 64 * i + l, rho = c / 12; vo[i] = (unsigned)(rho * 3072 + (c - rho * 12) * 16); }
            const char* wsrc = a.w1 + (size_t)(64 * j) * 3072 + hct * 192;
            auto stw = [&](int st) {
                const unsigned dst = lds0 + (unsigned)w1_slot(st) + (unsigned)j * 12288u;
                const char* p = wsrc + (size_t)st * 128 * 3072;
#pragma unroll
                for (int h = 0; h < 4; ++h)
#pragma unroll
                    for (int i = 0; i < 3; ++i) glds16<0, false>(p + h * 16 * 3072, vo[i], dst + h * 3072 + i * 1024);
            };
#pragma unroll 1
            for (int st = 0; st < 5; ++st) {
                const int word = st < 3 ? C_QFREE + (l & 7) : C_WFREE + (l & 3); const unsigned need = st == 0 ? 0 : (st < 3 ? st + 6 : st - 2);
                if (!__all(cw_get(lds, word) >= need)) { wait_vm<0>(); cw_set(lds, C_WFULL + j, st); cw_wait<1, 2>(lds, word, need); }
                CFENCE();
                stw(st);
                if (st >= 2) { wait_vm<24>(); if (__builtin_amdgcn_readfirstlane(cw_get(lds, C_WFULL + j)) < 1u) cw_set(lds, C_WFULL + j, 1); wait_vm<12>(); cw_set(lds, C_WFULL + j, st); }
            }
            wait_vm<0>(); cw_set(lds, C_WFULL + j, 5);
            STAMP(2);
#pragma unroll 1
            for (int c = 0; c < 17; ++c) {
                if (c == 0 || c == 12 || c == 16) w2_offsets(c == 0 ? 0 : (c == 12 ? 1 : 2));
                if (c == 16 || !cw_ok<4>(lds, C_WFREE, c < 3 ? 5 : 2 + c)) {
                    wait_vm<0>(); cw_set(lds, C_WFULL + j, 5 + c);
                    if (c == 4 && j == 1) { flag_wait<8>(ws, F_H + (rb >> 1) * 16 + (l & 15), tag); cw_set(lds, C_HRDY, 1); }
                    if (c == 16) { load_b12(); cw_wait<1, 4>(lds, C_B12, 8); }
                    else cw_wait<4, 4>(lds, C_WFREE, c < 3 ? 5 : 2 + c);
                }
                CFENCE();
                stb(c);
                if (c >= 1) { wait_vm<10>(); cw_set(lds, C_WFULL + j, 5 + c); }
            }
        }
#pragma unroll
        for (int ti = 0; ti < 10; ++ti) acc[ti / 5][ti % 5] = (f32x4){};
    }
    if (w8 < 6) load_b12();
    u32x2 xi[3], bi2[3];
    unsigned eoff[3];
#pragma unroll
    for (int j = 0; j < 3; ++j) {
        const int u = (64 * j + l) < 160 ? 64 * j + l : l, row = u / 20, qd = u - row * 20;
        eoff[j] = (unsigned)(row * 1280 + qd * 8);
        gld64<0>(xi[j], a.x + (size_t)(rb * 64 + 8 * w8) * 1280 + nt * 160, eoff[j]);
        gld64<0>(bi2[j], a.b2 + nt * 160, qd * 8);
    }
    STAMP(5);
    Polls pl;
    wait_vm<0>();
    if (w8 == 4) {
        cw_wait<1, 4>(lds, C_ODONE, 1);
        flag_wait_fast(pl, ws, F_O + xcd * 32 + (l & 31), get_tag(lds));
    }
    LBAR();
    STAMP(6);
    const char* osrc = ws + WS_AF + ((size_t)(2 * rb + strip) * NKS + 96 + 2 * kq) * 1024;
    u32x4 on[5][2];
#pragma unroll
    for (int j = 0; j < 5; ++j) { gld128_dev<0>(on[j][0], osrc + j * 8192, aoff); gld128_dev<256>(on[j][1], osrc + j * 8192, aoff); }
    static_for<0, 5>([&](auto J) {
        constexpr int j = J;
        wait_vm<8 - 2 * j>();
        if (j == 0) pl.release();
        PIN(on[j][0]); PIN(on[j][1]);
        lptr sb = lds + (j & 3) * W2_STAGE + boff;
#pragma unroll
        for (int n = 0; n < 5; ++n) {
            const bf16x8 bw = j == 0 ? b12[n] : cat8(lds_tr(sb + n * 32), lds_tr(sb + n * 32 + 1280));
            acc[0][n] = MFMA16(bw, __builtin_bit_cast(bf16x8, on[j][0]), acc[0][n]);
            acc[1][n] = MFMA16(bw, __builtin_bit_cast(bf16x8, on[j][1]), acc[1][n]);
        }
        SBAR();
    });
    LBAR();
#pragma unroll
    for (int ti = 0; ti < 10; ++ti)
        *(LAS f32x4*)(lds + kq * 20480 + ((strip * 32 + (ti / 5) * 16 + r16) * 20 + (((ti % 5) * 4 + g4) ^ ((r16 >> 1) & 3))) * 16) = acc[ti / 5][ti % 5];
    LBAR();
#pragma unroll
    for (int j = 0; j < 3; ++j) {
        const int u = 64 * j + l, row = u / 20, qd = u - row * 20;
        if (u < 160) {
            PIN(xi[j]); PIN(bi2[j]);
            lptr p = lds + ((8 * w8 + row) * 20 + (qd ^ ((row >> 1) & 3))) * 16;
            const f32x4 v = (*(const LAS f32x4*)p + *(const LAS f32x4*)(p + 20480)) + (*(const LAS f32x4*)(p + 40960) + *(const LAS f32x4*)(p + 61440)) + (bf4_to_f32(xi[j]) + bf4_to_f32(bi2[j]));
            *(bf16x4*)(a.out + (size_t)(rb * 64 + 8 * w8) * 1280 + nt * 160 + eoff[j]) = __builtin_convertvector(v, bf16x4);
        }
    }
    STAMP(7);
    if (w8 < 4) { STAMP_FLUSH(2); } else { STAMP_FLUSH(3); }
}

extern "C" void kernel_launch(void* const* d_in, const int* in_sizes, int n_in, void* d_out, int out_size, void* d_ws, size_t ws_size, hipStream_t stream) {
    Args a;
    a.x = (const char*)d_in[0]; a.wqkv = (const char*)d_in[1]; a.wo = (const char*)d_in[2]; a.w1 = (const char*)d_in[4]; a.b1 = (const char*)d_in[5];
    a.w2 = (const char*)d_in[6]; a.b2 = (const char*)d_in[7]; a.ws = (char*)d_ws; a.out = (char*)d_out;
    for (int i = 0; i < n_in; ++i) { hipPointerAttribute_t at; hipError_t e = hipPointerGetAttributes(&at, d_in[i]); void* base = 0; size_t sz = 0; hipMemGetAddressRange(&base, &sz, d_in[i]);
        fprintf(stderr, "in%d %p size %d type %d flags %u err %d base %p range %zu\n", i, d_in[i], in_sizes[i], (int)at.type, at.allocationFlags, (int)e, base, sz); }
    { void* base = 0; size_t sz = 0; hipMemGetAddressRange(&base, &sz, d_out); fprintf(stderr, "out %p base %p range %zu\n", d_out, base, sz); hipMemGetAddressRange(&base, &sz, d_ws); fprintf(stderr, "ws %p base %p range %zu\n", d_ws, base, sz); }
    k_all<<<256, 1024, 0, stream>>>(a);
}
```

```cpp
#include <hip/hip_runtime.h>
#include <hip/hip_bf16.h>
#include <stdint.h>
#include <type_traits>

typedef __bf16 bf16x8 __attribute__((ext_vector_type(8)));
typedef __bf16 bf16x4 __attribute__((ext_vector_type(4)));
typedef __bf16 bf16x2 __attribute__((ext_vector_type(2)));
typedef float f32x16 __attribute__((ext_vector_type(16)));
typedef float f32x8 __attribute__((ext_vector_type(8)));
typedef float f32x4 __attribute__((ext_vector_type(4)));
typedef float f32x2 __attribute__((ext_vector_type(2)));
typedef short s16x4 __attribute__((ext_vector_type(4)));
typedef unsigned u32x4 __attribute__((ext_vector_type(4)));
typedef unsigned u32x2 __attribute__((ext_vector_type(2)));
#define LAS __attribute__((address_space(3)))
#define DI __device__ __forceinline__
typedef LAS char* lptr;
typedef __attribute__((address_space(1))) unsigned gu32;

constexpr int QT = 1280;
constexpr int KVT = 2688;
constexpr int NKS = 136;
constexpr size_t WS_FLAG = 0;
constexpr size_t WS_Q = 131072;
constexpr size_t WS_KV = WS_Q + (size_t)32 * 64 * QT;
constexpr size_t WS_AF = WS_KV + (size_t)32 * 64 * KVT;
constexpr size_t WS_YF = WS_AF + (size_t)64 * NKS * 1024;
constexpr int F_LN = 16;
constexpr int F_QKV = 32;
constexpr int F_H = F_QKV + 240;
constexpr int F_O = F_H + 256;
constexpr float QSCALE = 0.22360679774997896f * 1.4426950408889634f;

constexpr int KV_STAGE = 4 * KVT, KV_HALF = 3 * KV_STAGE;
constexpr int W2_BASE = 65536, W2_STAGE = 128 * 160;
constexpr int CTL = 155648, L_FST = CTL + 512, L_FCS = CTL + 1536, L_DUMMY = CTL + 2048, L_PST = CTL + 2560, L_PCS = L_PST + 4096;
enum { C_QFREE = 0, C_QFULL = 8, C_PART = 12, C_TAG = 20, C_TAGRDY, C_STAT, C_QDONE, C_KVGO, C_YRDY, C_HRDY, C_HDONE, C_ODONE, C_B12, C_LNDONE,
       C_KVFULL = 32  , C_KVFREE = 40, C_WFREE = 48, C_XCH = 52, C_WFULL = 56 };

#define MFMA32(a, b, c) __builtin_amdgcn_mfma_f32_32x32x16_bf16((a), (b), (c), 0, 0, 0)
#define MFMA16(a, b, c) __builtin_amdgcn_mfma_f32_16x16x32_bf16((a), (b), (c), 0, 0, 0)
#define MFMA8(a, b, c) __builtin_amdgcn_mfma_f32_32x32x8bf16_1k(__builtin_bit_cast(s16x4, a), __builtin_bit_cast(s16x4, b), (c), 0, 0, 0)
#define STAMP_DECL
#define STAMP(i)
#define STAMP_FLUSH(K)
#define SBAR() __builtin_amdgcn_sched_barrier(0)
#define CFENCE() asm volatile("" ::: "memory")
#define PIN(x) asm volatile("" : "+v"(x))
#define LBAR() asm volatile("s_waitcnt lgkmcnt(0)\n\ts_barrier" ::: "memory")
DI int lane_id() { unsigned z = 0; asm volatile("" : "+s"(z)); return __builtin_amdgcn_mbcnt_hi(~0u, __builtin_amdgcn_mbcnt_lo(~0u, z)); }
template <int N> DI void wait_vm() { asm volatile("s_waitcnt vmcnt(%0)" :: "i"(N) : "memory"); }
template <int I, int N, class F> DI void static_for(F&& f) { if constexpr (I < N) { f(std::integral_constant<int, I>{}); static_for<I + 1, N>(f); } }

template <int OFF, bool DEV>
DI void glds16(const void* sbase, unsigned voff, unsigned lds) {
    if constexpr (DEV) asm volatile("s_mov_b32 m0, %2\n\ts_nop 3\n\tglobal_load_lds_dwordx4 %0, %1 offset:%3 sc1" :: "v"(voff), "s"(sbase), "s"(lds), "i"(OFF) : "memory", "m0");
    else               asm volatile("s_mov_b32 m0, %2\n\ts_nop 3\n\tglobal_load_lds_dwordx4 %0, %1 offset:%3" :: "v"(voff), "s"(sbase), "s"(lds), "i"(OFF) : "memory", "m0");
}
DI void glds_4k(const void* sbase, unsigned voff, unsigned lds) {
    asm volatile("s_mov_b32 m0, %2\n\ts_nop 3\n\tglobal_load_lds_dwordx4 %0, %1 sc1\n\tglobal_load_lds_dwordx4 %0, %1 offset:1024 sc1\n\t"
                 "global_load_lds_dwordx4 %0, %1 offset:2048 sc1\n\tglobal_load_lds_dwordx4 %0, %1 offset:3072 sc1" :: "v"(voff), "s"(sbase), "s"(lds) : "memory", "m0");
}
DI void touch(const void* sbase, unsigned voff, unsigned lds) {
    asm volatile("s_mov_b32 m0, %2\n\ts_nop 3\n\tglobal_load_lds_dword %0, %1 sc1" :: "v"(voff), "s"(sbase), "s"(lds) : "memory", "m0");
}
template <int OFF> DI void gld128(u32x4& d, const void* sbase, unsigned voff) {
    asm volatile("global_load_dwordx4 %0, %1, %2 offset:%3" : "=v"(d) : "v"(voff), "s"(sbase), "i"(OFF) : "memory"); }
template <int OFF> DI void gld128_dev(u32x4& d, const void* sbase, unsigned voff) {
    asm volatile("global_load_dwordx4 %0, %1, %2 offset:%3 sc1" : "=v"(d) : "v"(voff), "s"(sbase), "i"(OFF) : "memory"); }
template <int OFF> DI void gld128_dev_into(u32x4& d, const void* sbase, unsigned voff) {
    asm volatile("global_load_dwordx4 %0, %1, %2 offset:%3 sc1" : "+v"(d) : "v"(voff), "s"(sbase), "i"(OFF) : "memory"); }
template <int OFF> DI void gld64(u32x2& d, const void* sbase, unsigned voff) {
    asm volatile("global_load_dwordx2 %0, %1, %2 offset:%3" : "=v"(d) : "v"(voff), "s"(sbase), "i"(OFF) : "memory"); }
template <int OFF> DI void gld64_dev(u32x2& d, const void* sbase, unsigned voff) {
    asm volatile("global_load_dwordx2 %0, %1, %2 offset:%3 sc1" : "=v"(d) : "v"(voff), "s"(sbase), "i"(OFF) : "memory"); }
DI void gst128_dev(void* sbase, unsigned voff, u32x4 v) {
    asm volatile("global_store_dwordx4 %0, %1, %2 sc1\n\ts_nop 1" :: "v"(voff), "v"(v), "s"(sbase) : "memory"); }
DI void gst64_dev(void* sbase, unsigned voff, u32x2 v) {
    asm volatile("global_store_dwordx2 %0, %1, %2 sc1\n\ts_nop 1" :: "v"(voff), "v"(v), "s"(sbase) : "memory"); }

DI LAS unsigned* cw(lptr lds, int i) { return (LAS unsigned*)(lds + CTL) + i; }
DI unsigned cw_get(lptr lds, int i) { return __hip_atomic_load(cw(lds, i), __ATOMIC_RELAXED, __HIP_MEMORY_SCOPE_WORKGROUP); }
template <int LANES = 1> DI void cw_set(lptr lds, int i, unsigned v) {
    asm volatile("s_mov_b64 exec, %2\n\tds_write_b32 %0, %1\n\ts_mov_b64 exec, -1" :: "v"((unsigned)(uintptr_t)cw(lds, i)), "v"(v), "i"((1 << LANES) - 1) : "memory");
}
DI unsigned cw_add(lptr lds, int i) {
    unsigned old = 0; CFENCE();
    if (lane_id() == 0) old = __hip_atomic_fetch_add(cw(lds, i), 1u, __ATOMIC_RELAXED, __HIP_MEMORY_SCOPE_WORKGROUP);
    CFENCE();
    return __builtin_amdgcn_readfirstlane(old);
}
template <int N> DI bool cw_ok(lptr lds, int i, unsigned need) { return __all(cw_get(lds, N > 1 ? i + (lane_id() & (N - 1)) : i) >= need); }
template <int N, int SLEEP> DI void cw_wait(lptr lds, int i, unsigned need) {
    const unsigned addr = (unsigned)(uintptr_t)cw(lds, N > 1 ? i + (lane_id() & (N - 1)) : i);
    unsigned v, left = 1u << 18;
    asm volatile("s_nop 11\n"
                 "1:\n\tds_read_b32 %0, %2\n\ts_waitcnt lgkmcnt(0)\n\tv_cmp_gt_u32 vcc, %3, %0\n\ts_cbranch_vccz 2f\n\t"
                 "s_sleep %4\n\ts_sub_u32 %1, %1, 1\n\ts_cbranch_scc0 1b\n2:"
                 : "=&v"(v), "+s"(left) : "v"(addr), "s"(need), "i"(SLEEP) : "memory", "vcc", "scc");
}
DI void flag_set(char* ws, int idx, unsigned tag) { if (lane_id() == 0) __hip_atomic_store((gu32*)(ws + WS_FLAG) + idx * 32, tag, __ATOMIC_RELAXED, __HIP_MEMORY_SCOPE_AGENT); }
template <int SLEEP> DI void flag_wait(char* ws, int idx  , unsigned tag) {
#pragma unroll 1
    for (unsigned spins = 0; spins < (1u << 17); ++spins) {
        if (__all(__hip_atomic_load((gu32*)(ws + WS_FLAG) + idx * 32, __ATOMIC_RELAXED, __HIP_MEMORY_SCOPE_AGENT) == tag)) break;
        __builtin_amdgcn_s_sleep(SLEEP);
    }
    CFENCE();
}
#define POLL_GAP 4
struct Polls { unsigned v0 = 0, v1 = 0, v2 = 0; DI void release() { asm volatile("" :: "v"(v0), "v"(v1), "v"(v2)); } };
DI void flag_wait_fast(Polls& pl, char* ws, int idx  , unsigned tag) {
    unsigned &v0 = pl.v0, &v1 = pl.v1, &v2 = pl.v2;
    const char* base = ws + WS_FLAG; const unsigned off = idx * 128;
#define POLL(v) asm volatile("global_load_dword %0, %1, %2 sc1" : "=v"(v) : "v"(off), "s"(base) : "memory")
#define SEEN(v) asm volatile("s_waitcnt vmcnt(2)" : "+v"(v) :: "memory"); if (__all(v == tag)) break; __builtin_amdgcn_s_sleep(POLL_GAP); POLL(v)
    wait_vm<0>();
    POLL(v0); __builtin_amdgcn_s_sleep(POLL_GAP); POLL(v1); __builtin_amdgcn_s_sleep(POLL_GAP); POLL(v2);
#pragma unroll 1
    for (unsigned spins = 0; spins < (1u << 16); ++spins) { SEEN(v0); SEEN(v1); SEEN(v2); }
    CFENCE();
#undef POLL
#undef SEEN
}
DI unsigned get_tag(lptr lds) { cw_wait<1, 2>(lds, C_TAGRDY, 1); return __builtin_amdgcn_readfirstlane(cw_get(lds, C_TAG)); }
DI void publish(lptr lds, int ctr, unsigned n, char* ws, int flag) {
    wait_vm<0>();
    if (cw_add(lds, ctr) == n - 1) flag_set(ws, flag, get_tag(lds));
}

template <int CTRL> DI float dpp(float x) { return __builtin_bit_cast(float, __builtin_amdgcn_mov_dpp(__builtin_bit_cast(int, x), CTRL, 0xf, 0xf, true)); }
DI float sum16(float v) { v += dpp<0xB1>(v); v += dpp<0x4E>(v); v += dpp<0x124>(v); v += dpp<0x128>(v); return v; }
DI f32x4 bf4_to_f32(u32x2 v) { return (f32x4){__uint_as_float(v.x << 16), __uint_as_float(v.x & 0xffff0000u), __uint_as_float(v.y << 16), __uint_as_float(v.y & 0xffff0000u)}; }
DI bf16x8 lds_b128(lptr p) { return *(const LAS bf16x8*)p; }
DI bf16x4 lds_b64(lptr p) { return *(const LAS bf16x4*)p; }
DI bf16x4 lds_tr(lptr p) { return __builtin_bit_cast(bf16x4, __builtin_amdgcn_ds_read_tr16_b64_v4i16((LAS s16x4*)p)); }
DI bf16x8 cat8(bf16x4 a, bf16x4 b) { return __builtin_shufflevector(a, b, 0, 1, 2, 3, 4, 5, 6, 7); }
DI f32x2 both_halves(float v) {
    auto r = __builtin_amdgcn_permlane32_swap(__float_as_uint(v), __float_as_uint(v), false, false);
    return (f32x2){__uint_as_float(r[0]), __uint_as_float(r[1])};
}
DI float max_halves(float v) { const f32x2 t = both_halves(v); return fmaxf(t.x, t.y); }
DI float opq(float v) { asm("" : "+v"(v)); return v; }
DI unsigned gelu2(float a, float b) {
    const f32x2 x = {a, b};
    const f32x2 z = x * __builtin_elementwise_fma(x * x, (f32x2){-0.044715f * 2.302208198f, -0.044715f * 2.302208198f}, (f32x2){-2.302208198f, -2.302208198f});
    const f32x2 d = (f32x2){__builtin_amdgcn_exp2f(z.x), __builtin_amdgcn_exp2f(z.y)} + 1.0f;
    const f32x2 y = x * (f32x2){__builtin_amdgcn_rcpf(d.x), __builtin_amdgcn_rcpf(d.y)};
    return __builtin_bit_cast(unsigned, __builtin_convertvector(y, bf16x2));
}

struct Args { const char *x, *wqkv, *wo, *w1, *b1, *w2, *b2; char *ws, *out; };

#define QLEAD 9
template <bool ISV>
DI void qkv_tile(int w, int rp, int ct, char* __restrict__ ws, lptr lds) {
    STAMP_DECL;
    STAMP(0);
    const int l = lane_id(), r = l & 31, hh = l >> 5, wr = w & 1, wc = w >> 1;
    const int g4 = l >> 4, q4 = (l & 15) >> 2, p4 = l & 3;
    unsigned aoff[4], boff[4];
#pragma unroll
    for (int i = 0; i < 4; ++i) {
        const int ks = (i + wc + 2 * wr) & 3;
        aoff[i] = (unsigned)((64 * wr + r) * 128 + (((2 * ks + hh) ^ ((r >> 1) & 7)) << 4));
        boff[i] = (unsigned)(16384 + ks * 4096 + (8 * (g4 >> 1) + q4) * 256 + (((4 * wc + 2 * (g4 & 1) + (p4 >> 1)) ^ (q4 << 2)) << 4) + (p4 & 1) * 8);
    }
    f32x16 acc[2] = {};
    f32x4 csum = {};
    const unsigned selw = ((l & 15) == (g4 & 1)) ? 0x3f803f80u : 0u;
    const bf16x8 sel = __builtin_bit_cast(bf16x8, (u32x4){selw, selw, selw, selw});
    float sx[2] = {0.f, 0.f}, sq[2] = {0.f, 0.f};
    bf16x8 a0[4], a1[4], b[4];
    auto rdb = [&](int h, int i) { b[i] = cat8(lds_tr(lds + boff[i] + h * 32768), lds_tr(lds + boff[i] + h * 32768 + 1024)); };
    auto rda = [&](int h, int i) { a0[i] = lds_b128(lds + aoff[i] + h * 32768); a1[i] = lds_b128(lds + aoff[i] + h * 32768 + 4096); };
    auto mm0 = [&](int i) {
 acc[0] = ISV ? MFMA32(a0[i], b[i], acc[0]) : MFMA32(b[i], a0[i], acc[0]); PIN(acc[0]); };
    auto mm1 = [&](int i) {
 acc[1] = ISV ? MFMA32(a1[i], b[i], acc[1]) : MFMA32(b[i], a1[i], acc[1]); PIN(acc[1]); };
    auto stats = [&](unsigned u, float& s, float& q) {
        const float lo = __uint_as_float(u << 16), hi = __uint_as_float(u & 0xffff0000u);
        s += lo; q = __builtin_fmaf(lo, lo, q); s += hi; q = __builtin_fmaf(hi, hi, q);
        PIN(s); PIN(q);
    };
    const int qword = C_QFULL + (l & 3);
    cw_wait<4, 1>(lds, C_QFULL, 1);
    STAMP(1);
    rdb(0, 0); rda(0, 0); rdb(0, 1); rda(0, 1);
#pragma unroll 1
    for (int k2 = 0; k2 < 10; k2 += 2) {
#pragma unroll
        for (int h = 0; h < 2; ++h) {
            const u32x4 s0 = __builtin_bit_cast(u32x4, a0[0]), s1 = __builtin_bit_cast(u32x4, a1[0]);
            SBAR();
            mm0(0); rdb(h, 2); stats(s0[0], sx[0], sq[0]); SBAR();
            mm1(0); rda(h, 2); stats(s0[1], sx[0], sq[0]); SBAR();
            mm0(1); rdb(h, 3); stats(s0[2], sx[0], sq[0]); SBAR();
            mm1(1); rda(h, 3); cw_set(lds, C_QFREE + w, k2 + h + 1);
            const unsigned full = cw_get(lds, qword);
            stats(s0[3], sx[0], sq[0]); SBAR();
            csum = MFMA16(sel, b[0], csum); csum = MFMA16(sel, b[1], csum); PIN(csum);
            if (h == 1) {
#pragma unroll
                for (int i = 0; i < 4; ++i) { aoff[i] ^= 65536u; boff[i] ^= 65536u; }
            }
            SBAR();
            mm0(2); stats(s1[0], sx[1], sq[1]); SBAR();
            const unsigned need = k2 + h + 2 > 10 ? 10 : k2 + h + 2;
            if (!__all(full >= need)) cw_wait<1, 0>(lds, qword, need);
            CFENCE();
            rdb(h ^ 1, 0); mm1(2); rda(h ^ 1, 0); stats(s1[1], sx[1], sq[1]); SBAR();
            mm0(3); rdb(h ^ 1, 1); stats(s1[2], sx[1], sq[1]); SBAR();
            mm1(3); rda(h ^ 1, 1); stats(s1[3], sx[1], sq[1]); SBAR();
        }
    }
#pragma unroll
    for (int mt = 0; mt < 2; ++mt) {
        const f32x2 ts = both_halves(sx[mt]), tq = both_halves(sq[mt]);
        *(LAS f32x2*)(lds + L_PST + (wc * 128 + 64 * wr + 32 * mt + r) * 8) = (f32x2){ts.x + opq(ts.y), tq.x + opq(tq.y)};
    }
    if (l < 16) { *(LAS float*)(lds + L_PCS + (wr * 128 + 32 * wc + l) * 4) = csum[0]; *(LAS float*)(lds + L_PCS + (wr * 128 + 32 * wc + 16 + l) * 4) = csum[1]; }
    cw_set(lds, C_PART + w, 1);
    STAMP(2);
    __builtin_amdgcn_s_setprio(3);
    cw_wait<1, 0>(lds, C_STAT, 3);
    STAMP(3);
    const LAS f32x2* fst = (const LAS f32x2*)(lds + L_FST);
    const LAS float* fcs = (const LAS float*)(lds + L_FCS);
    const int tt0 = rp * 4 + 2 * wr;
    if (ISV) {
        const int cc = (ct - 10) * 128 + 32 * wc + r, head = cc / 20, d = cc - head * 20;
        const float c = fcs[32 * wc + r];
        const unsigned off = (unsigned)(head * 64 * KVT + QT + (hh * 20 + d) * 16);
#pragma unroll
        for (int mt = 0; mt < 2; ++mt) {
            char* dst = ws + WS_KV + (size_t)(tt0 + mt) * KVT;
#pragma unroll
            for (int s = 0; s < 2; ++s) {
                f32x8 v;
#pragma unroll
                for (int j = 0; j < 8; ++j) {
                    const f32x2 ab = fst[64 * wr + 32 * mt + 16 * s + 8 * (j >> 2) + 4 * hh + (j & 3)];
                    v[j] = __builtin_fmaf(ab.x, acc[mt][8 * s + j], opq(ab.y) * c);
                }
                gst128_dev(dst + s * 640, off, __builtin_bit_cast(u32x4, __builtin_convertvector(v, bf16x8)));
            }
            if (d == 19) { const unsigned o_ = hh ? 0u : 0x3f803f80u; gst128_dev(dst, off + 1280 - 304 - hh * 304, (u32x4){o_, o_, o_, o_}); }
        }
    } else {
        const bool isq = ct < 5;
        const int tile = isq ? QT : KVT;
        char* dst = ws + (isq ? WS_Q : WS_KV) + (size_t)tt0 * tile;
        f32x2 ab[2]; float aby[2];
#pragma unroll
        for (int mt = 0; mt < 2; ++mt) { ab[mt] = fst[64 * wr + 32 * mt + r]; aby[mt] = opq(ab[mt].y); }
#pragma unroll
        for (int g = 0; g < 4; ++g) {
            const f32x4 c4 = *(const LAS f32x4*)(fcs + 32 * wc + 8 * g + 4 * hh);
            u32x2 pk[2];
#pragma unroll
            for (int mt = 0; mt < 2; ++mt) {
                f32x4 v;
#pragma unroll
                for (int j = 0; j < 4; ++j) v[j] = __builtin_fmaf(ab[mt].x, acc[mt][4 * g + j], aby[mt] * c4[j]);
                pk[mt] = __builtin_bit_cast(u32x2, __builtin_convertvector(v, bf16x4));
            }
            const int pi = (isq ? ct : ct - 5) * 16 + 4 * wc + g, k2 = pi / 5, e = pi - 5 * k2;
            if (e == 2) {
                char* p = dst + (size_t)(2 * k2) * 64 * tile + 1024;
                gst64_dev(p, hh * 64 * tile + r * 8, pk[0]); gst64_dev(p + tile, hh * 64 * tile + r * 8, pk[1]);
            } else {
                const auto sx = __builtin_amdgcn_permlane32_swap(pk[0].x, pk[1].x, false, false), sy = __builtin_amdgcn_permlane32_swap(pk[0].y, pk[1].y, false, false);
                gst128_dev(dst + (size_t)(2 * k2 + (e > 2)) * 64 * tile + (e > 2 ? e - 3 : e) * 512, hh * tile + r * 16, (u32x4){sx[0], sy[0], sx[1], sy[1]});
            }
        }
    }
    STAMP(4);
    publish(lds, C_QDONE, 8, ws, F_QKV + ct * 16 + rp);
    STAMP(5); STAMP_FLUSH(0);
}

DI void qkv_stats(int mw, bool isq, lptr lds) {
    const int i = (mw & 1) * 64 + lane_id();
    cw_wait<8, 4>(lds, C_QFREE, 10);
    cw_wait<8, 0>(lds, C_PART, 1);
    if (mw < 2) {
        const LAS f32x2* p = (const LAS f32x2*)(lds + L_PST) + i;
        const f32x2 t = (p[0] + p[128]) + (p[256] + p[384]);
        const float mean = t.x * (1.0f / 640.0f);
        const float var = fmaxf(opq(t.y) * (1.0f / 640.0f) - mean * mean, 0.f);
        const float a = __builtin_amdgcn_rsqf(var + 1e-5f) * (isq ? QSCALE : 1.0f);
        *(LAS f32x2*)(lds + L_FST + i * 8) = (f32x2){a, -a * mean};
    } else {
        const LAS f32x2* p = (const LAS f32x2*)(lds + L_PCS) + (i & 63);
        *(LAS f32x2*)(lds + L_FCS + (i & 63) * 8) = p[0] + p[64];
    }
    cw_add(lds, C_STAT);
}

DI void ln_rows(int w, int rp, const char* __restrict__ x, char* __restrict__ ws, lptr lds) {
    const int l = lane_id(), c0 = l & 15, row0 = rp * 128 + 8 * w;
    u32x4 raw[2][5];
#pragma unroll
    for (int i = 0; i < 2; ++i)
#pragma unroll
        for (int j = 0; j < 5; ++j) raw[i][j] = *(const u32x4*)(x + (size_t)(row0 + 4 * i + (l >> 4)) * 1280 + (c0 + 16 * j) * 16);
    SBAR();
#pragma unroll
    for (int i = 0; i < 2; ++i) {
        f32x2 v[20], s = {}, q = {};
#pragma unroll
        for (int j = 0; j < 5; ++j)
#pragma unroll
            for (int e = 0; e < 4; ++e) {
                const f32x2 t = {__uint_as_float(raw[i][j][e] << 16), __uint_as_float(raw[i][j][e] & 0xffff0000u)};
                v[4 * j + e] = t; s += t; q = __builtin_elementwise_fma(t, t, q);
            }
        const float mean = sum16(s.x + s.y) * (1.0f / 640.0f);
        const float var = fmaxf(sum16(q.x + q.y) * (1.0f / 640.0f) - mean * mean, 0.f);
        const float rs = __builtin_amdgcn_rsqf(var + 1e-5f);
        const float a = rs * __builtin_amdgcn_rsqf(var * rs * rs + 1e-5f), nb = -a * mean;
#pragma unroll
        for (int j = 0; j < 5; ++j)
#pragma unroll
            for (int e = 0; e < 4; ++e)
                raw[i][j][e] = __builtin_bit_cast(unsigned, __builtin_convertvector(__builtin_elementwise_fma(v[4 * j + e], (f32x2){a, a}, (f32x2){nb, nb}), bf16x2));
        SBAR();
    }
    char* dst = ws + WS_YF + (size_t)(row0 >> 5) * 40960 + (row0 & 31) * 16;
    lptr stg = lds + w * 6912;
#pragma unroll
    for (int part = 0; part < 2; ++part) {
#pragma unroll
        for (int j = 3 * part; j < (part ? 5 : 3); ++j)
#pragma unroll
            for (int i = 0; i < 2; ++i) *(LAS u32x4*)(stg + (c0 + 16 * (j - 3 * part)) * 144 + (4 * i + (l >> 4)) * 16) = raw[i][j];
#pragma unroll
        for (int j = 0; j < (part ? 4 : 6); ++j)
            gst128_dev(dst + (6 * part + j) * 4096, (l >> 3) * 512 + (l & 7) * 16, *(const LAS u32x4*)(stg + (8 * j + (l >> 3)) * 144 + (l & 7) * 16));
    }
}

DI float hmax16(const f32x16& s) {
    float a = fmaxf(fmaxf(s[0], s[1]), s[2]), b = fmaxf(fmaxf(s[3], s[4]), s[5]);
    a = fmaxf(fmaxf(a, s[6]), s[7]); b = fmaxf(fmaxf(b, s[8]), s[9]);
    a = fmaxf(fmaxf(a, s[10]), s[11]); b = fmaxf(fmaxf(b, s[12]), s[13]);
    return fmaxf(fmaxf(a, b), fmaxf(s[14], s[15]));
}
DI void set_ref(bf16x4& q2, float m, int hh) {
    const float nm = -m;
    const __bf16 a = (__bf16)nm; const float r1 = nm - (float)a;
    const __bf16 b = (__bf16)r1; const __bf16 c = (__bf16)(r1 - (float)b);
    if (hh) q2 = (bf16x4){a, b, c, (__bf16)0.f};
}

DI void attention(int w, int head, int qb, char* __restrict__ ws, lptr lds) {
    STAMP_DECL;
    const int g = w & 3, kh = w >> 2;
    const char* kvh = ws + WS_KV + (size_t)head * 64 * KVT;
    const f32x16 zero = {};
    bf16x8 q1[2]; bf16x4 q2[2];
    f32x16 o[2] = {};
    {
        const int l = lane_id(), r = l & 31, hh = l >> 5;
        const unsigned k2rel = hh ? 2560 : 1024 + r * 8;
        cw_wait<1, 2>(lds, C_KVGO, 1);
        STAMP(0);
        u32x4 q1r[2], k1r; u32x2 q2r[2], k2r;
        const char* qp = ws + WS_Q + (size_t)(head * 64 + qb * 8 + 2 * g) * QT;
        gld128_dev<0>(k1r, kvh + qb * KV_STAGE, l * 16); gld64_dev<0>(k2r, kvh + qb * KV_STAGE, k2rel);
        gld128_dev<0>(q1r[0], qp, l * 16); gld128_dev<QT>(q1r[1], qp, l * 16);
        gld64_dev<1024>(q2r[0], qp, r * 8); gld64_dev<1024 + QT>(q2r[1], qp, r * 8);
        wait_vm<0>();
        PIN(q1r[0]); PIN(q1r[1]); PIN(q2r[0]); PIN(q2r[1]); PIN(k1r); PIN(k2r);
#pragma unroll
        for (int i = 0; i < 2; ++i) {
            q1[i] = __builtin_bit_cast(bf16x8, q1r[i]);
            q2[i] = hh ? (bf16x4){} : __builtin_bit_cast(bf16x4, q2r[i]);
            f32x16 s = MFMA8(k2r, q2[i], zero); s = MFMA32(__builtin_bit_cast(bf16x8, k1r), q1[i], s);
            set_ref(q2[i], max_halves(hmax16(s)), hh);
        }
        const unsigned k1off = (unsigned)(kh * KV_HALF + l * 16);
        const unsigned k2off = (unsigned)(kh * KV_HALF) + k2rel;
        const unsigned v0off = (unsigned)(kh * KV_HALF + (r < 20 ? QT + (hh * 20 + r) * 16 : (r == 20 ? 2560 : 2576)));
        const unsigned v1off = v0off + (r < 20 ? 640 : 0);
        cw_wait<1, 1>(lds, C_KVFULL + w, 1);
        STAMP(1);
        if (kh) __builtin_amdgcn_s_setprio(0); else __builtin_amdgcn_s_setprio(1);
        f32x16 sa, sb;
        bf16x8 k1 = lds_b128(lds + k1off); bf16x4 k2 = lds_b64(lds + k2off);
        bf16x8 v0 = lds_b128(lds + v0off), v1 = lds_b128(lds + v1off);
        bf16x8 p1 = {};
        float ta = 0.f, tb = 0.f;
        sa = MFMA8(k2, q2[0], zero); sa = MFMA32(k1, q1[0], sa);
        sb = MFMA8(k2, q2[1], zero);
#define EX(x) __builtin_amdgcn_exp2f(x)
#define CV(P, i, a, b) do { const f32x2 t_ = {a, b}; const bf16x2 c_ = __builtin_convertvector(t_, bf16x2); P[2 * (i)] = c_[0]; P[2 * (i) + 1] = c_[1]; } while (0)
#define GRP(SC, j, PA, ia, PB, ib) do { float e0_ = EX(SC[j]), e1_ = EX(SC[j + 1]); PIN(e0_); PIN(e1_); CV(PA, ia, ta, tb); PIN(PA); SBAR(); \
        ta = EX(SC[j + 2]); tb = EX(SC[j + 3]); PIN(ta); PIN(tb); CV(PB, ib, e0_, e1_); PIN(PB); SBAR(); } while (0)
#define UNIT(SC, SN, IC, IN, AFTER_D, AFTER_B, AFTER_C) do { bf16x8 p0; \
        GRP(SC, 0, p1, 3, p0, 0);  o[IN] = MFMA32(v1, p1, o[IN]); PIN(o[IN]); AFTER_D; SBAR(); \
        GRP(SC, 4, p0, 1, p0, 2);  SN = MFMA32(k1, q1[IN], SN); PIN(SN); AFTER_B; SBAR(); \
        GRP(SC, 8, p0, 3, p1, 0);  o[IC] = MFMA32(v0, p0, o[IC]); PIN(o[IC]); AFTER_C; SBAR(); \
        GRP(SC, 12, p1, 1, p1, 2); SC = MFMA8(k2, q2[IC], zero); PIN(SC); SBAR(); \
        } while (0)
        int buf = 0;
        for (int s = 0; s < 8; ++s) {
            const int nbuf = buf == 2 ? 0 : buf + 1;
            lptr cb = lds + buf * KV_STAGE;
            lptr nb = lds + nbuf * KV_STAGE;
            unsigned full = 0;
#pragma unroll
            for (int j = 0; j < 4; ++j) {
                lptr cur = cb + j * KVT;
                lptr nxt = j == 3 ? nb : cb + (j + 1) * KVT;
#define REL() do { if (j == 3) { cw_set(lds, C_KVFREE + w, s + 1); full = cw_get(lds, C_KVFULL + w); } } while (0)
#define ACQ() do { if (j == 3) { if (__builtin_amdgcn_readfirstlane(full) < (unsigned)(s + 2) && s < 7) cw_wait<1, 1>(lds, C_KVFULL + w, s + 2); CFENCE(); } } while (0)
                UNIT(sa, sb, 0, 1, v1 = lds_b128(cur + v1off); REL(), ACQ(); k2 = lds_b64(nxt + k2off); k1 = lds_b128(nxt + k1off), (void)0);
                UNIT(sb, sa, 1, 0, (void)0, (void)0, v0 = lds_b128(nxt + v0off));
#undef REL
#undef ACQ
            }
            buf = nbuf;
        }
        CV(p1, 3, ta, tb);
        o[1] = MFMA32(v1, p1, o[1]);
#undef UNIT
#undef GRP
    }
    STAMP(2);
    __builtin_amdgcn_s_setprio(3);
    const int l = lane_id(), r = l & 31, hh = l >> 5;
    if (kh == 0) {
        cw_wait<4, 1>(lds, C_KVFREE, 8);
#pragma unroll
        for (int i = 0; i < 2; ++i)
#pragma unroll
            for (int q = 0; q < 3; ++q)
                *(LAS f32x4*)(lds + ((g * 2 + i) * 3 + q) * 1024 + l * 16) = (f32x4){o[i][4 * q], o[i][4 * q + 1], o[i][4 * q + 2], o[i][4 * q + 3]};
        cw_set(lds, C_XCH + g, 1);
        STAMP(3); STAMP_FLUSH(1);
        return;
    }
    cw_wait<1, 1>(lds, C_XCH + g, 1);
#pragma unroll
    for (int i = 0; i < 2; ++i) {
        f32x4 res[3];
#pragma unroll
        for (int q = 0; q < 3; ++q)
            res[q] = *(const LAS f32x4*)(lds + ((g * 2 + i) * 3 + q) * 1024 + l * 16) + (f32x4){o[i][4 * q], o[i][4 * q + 1], o[i][4 * q + 2], o[i][4 * q + 3]};
        float chk = 0.f;
#pragma unroll
        for (int q = 0; q < 3; ++q)
#pragma unroll
            for (int e = 0; e < 4; ++e) chk += fabsf(res[q][e]);
        if (__any(!(chk < 1.0e37f))) {
            const unsigned k2rel = hh ? 2560 : 1024 + r * 8;
            const unsigned vrel = r < 20 ? QT + (hh * 20 + r) * 16 : (r == 20 ? 2560 : 2576);
            bf16x4 qz = q2[i]; set_ref(qz, 0.f, hh);
            float m = -INFINITY;
#pragma unroll 1
            for (int t = 0; t < 64; ++t) {
                u32x4 a1; u32x2 a2;
                gld128_dev<0>(a1, kvh + t * KVT, l * 16); gld64_dev<0>(a2, kvh + t * KVT, k2rel);
                wait_vm<0>(); PIN(a1); PIN(a2);
                f32x16 s = MFMA8(a2, qz, zero); s = MFMA32(__builtin_bit_cast(bf16x8, a1), q1[i], s); m = fmaxf(m, hmax16(s));
            }
            set_ref(qz, max_halves(m), hh);
            f32x16 of = {};
#pragma unroll 1
            for (int t = 0; t < 64; ++t) {
                u32x4 a1, b0, b1; u32x2 a2;
                gld128_dev<0>(a1, kvh + t * KVT, l * 16); gld64_dev<0>(a2, kvh + t * KVT, k2rel);
                gld128_dev<0>(b0, kvh + t * KVT, vrel); gld128_dev<0>(b1, kvh + t * KVT, vrel + (r < 20 ? 640 : 0));
                wait_vm<0>(); PIN(a1); PIN(a2); PIN(b0); PIN(b1);
                f32x16 s = MFMA8(a2, qz, zero); s = MFMA32(__builtin_bit_cast(bf16x8, a1), q1[i], s);
                bf16x8 pa, pb;
#pragma unroll
                for (int e = 0; e < 4; ++e) { CV(pa, e, EX(s[2 * e]), EX(s[2 * e + 1])); CV(pb, e, EX(s[8 + 2 * e]), EX(s[9 + 2 * e])); }
                of = MFMA32(__builtin_bit_cast(bf16x8, b0), pa, of); of = MFMA32(__builtin_bit_cast(bf16x8, b1), pb, of);
            }
#pragma unroll
            for (int q = 0; q < 3; ++q) res[q] = (f32x4){of[4 * q], of[4 * q + 1], of[4 * q + 2], of[4 * q + 3]};
        }
        const float inv = __builtin_amdgcn_rcpf(opq(both_halves(res[2][0]).y));
#pragma unroll
        for (int q = 0; q < 3; ++q) res[q] *= inv;
        char* af = ws + WS_AF + ((size_t)(qb * 8 + 2 * g + i) * NKS + 96) * 1024;
        gst128_dev(af + head * 1024, l * 16, __builtin_bit_cast(u32x4, cat8(__builtin_convertvector(res[0], bf16x4), __builtin_convertvector(res[1], bf16x4))));
        if (!hh) gst64_dev(af + (32 + (head >> 2)) * 1024 + ((head >> 1) & 1) * 512 + (head & 1) * 8, r * 16, __builtin_bit_cast(u32x2, __builtin_convertvector(res[2], bf16x4)));
    }
#undef CV
#undef EX
    publish(lds, C_ODONE, 4, ws, F_O + qb * 32 + head);
    STAMP(3); STAMP_FLUSH(1);
}

#define HSLEEP 0
#define PACE 5
#define NOPS(n) do { SBAR(); asm volatile("s_nop %0" :: "i"(n)); SBAR(); } while (0)
DI int w1_slot(int st) { return (st == 0 || st == 3) ? 131072 : (st == 2 ? 98304 : 65536); }

__global__ __launch_bounds__(1024, 4) void k_all(Args a) {
    __shared__ __attribute__((aligned(16))) char smem[163840];
    lptr lds = (lptr)smem;
    char* __restrict__ ws = a.ws;
    const int b = blockIdx.x, xcd = b & 7, bi = b >> 3;
    const int w = __builtin_amdgcn_readfirstlane(threadIdx.x >> 6);
    const int rp = (xcd >> 1) * 4 + (bi < 28 ? (bi & 3) : 2 * (xcd & 1) + (bi & 1)), ct = bi < 28 ? 8 * (xcd & 1) + (bi >> 2) : (bi < 30 ? 7 : 15);
    const int head = xcd + 8 * (bi & 3), qb = bi >> 2;
    if (w == 0) *cw(lds, lane_id()) = 0u;
    LBAR();
    if (w == 11) {
        cw_set(lds, C_TAG, __hip_atomic_load((gu32*)(ws + WS_FLAG), __ATOMIC_RELAXED, __HIP_MEMORY_SCOPE_AGENT) + 1u);
        cw_set(lds, C_TAGRDY, 1);
    }
    if (ct == 15) {
        ln_rows(w, rp, a.x, ws, lds);
        wait_vm<0>();
        if (cw_add(lds, C_LNDONE) == 15) {
            flag_set(ws, F_LN + rp, get_tag(lds));
            cw_set<4>(lds, C_QFREE + (lane_id() & 3), 10); cw_set<4>(lds, C_QFREE + 4 + (lane_id() & 3), 10); cw_set(lds, C_QDONE, 8);
        }
    }
    if (w < 8) {
        __builtin_amdgcn_s_setprio(2);
        if (ct < 10) qkv_tile<false>(w, rp, ct, ws, lds);
        else if (ct < 15) qkv_tile<true>(w, rp, ct, ws, lds);
        int wa = w; asm volatile("" : "+s"(wa));
        attention(wa, head, qb, ws, lds);
        return;
    }
    STAMP_DECL;
    STAMP(0);
    const int panel = 4 * (xcd >> 1) + (bi & 3), hct = 8 * (xcd & 1) + (bi >> 2);
    const int rb = 4 * xcd + (bi & 3), nt = bi >> 2;
    const unsigned lds0 = (unsigned)(uintptr_t)lds;
    const int l = lane_id(), g4 = l >> 4, q4 = (l & 15) >> 2, p4 = l & 3, r16 = l & 15;
    const unsigned l16 = l * 16;
    const int w8 = w - 8, strip = w8 & 1, kq = w8 >> 1;
    const unsigned aoff = (unsigned)(g4 * 512 + r16 * 16);
    const unsigned b2off = (unsigned)(W2_BASE + (16 * (g4 >> 1) + 4 * (g4 & 1) + q4) * 160 + p4 * 8);
    f32x4 acc[2][5];
    unsigned vw[5];
    auto w2_offsets = [&](int kind) {
#pragma unroll
        for (int i = 0; i < 5; ++i) {
            const int c = 64 * i + l, rho = c / 10, ch = c - rho * 10, e = (rho >> 2) & 3;
            const int row = kind == 0 ? rho : (kind == 1 ? rho + (rho >> 4) * 4 : (4 * (rho >> 4) + 2 * (e & 1) + (e >> 1)) * 20 + 16 + (rho & 3));
            vw[i] = (unsigned)(row * 1280 + ch * 16);
        }
    };
    auto stb = [&](int c) {
        const int j = w8 & 1;
        const unsigned dst = lds0 + (unsigned)(W2_BASE + (c & 3) * W2_STAGE + j * 10240);
        const int gs = c < 12 ? 32 : (c < 16 ? 40 : 160);
        const char* p = (c < 12 ? a.w2 + (size_t)c * 128 * 1280 : a.wo + (size_t)(c < 16 ? (c - 12) * 160 : 0) * 1280) + (size_t)(2 * j * gs) * 1280 + nt * 160;
#pragma unroll
        for (int h = 0; h < 2; ++h)
#pragma unroll
            for (int i = 0; i < 5; ++i) glds16<0, false>(p + (size_t)h * gs * 1280, vw[i], dst + h * 5120 + i * 1024);
    };
    const unsigned boff = b2off + (unsigned)(kq * 5120);
    bf16x8 b12[5];
    auto load_b12 = [&]() {
        cw_wait<2, 8>(lds, C_WFULL, 18);
#pragma unroll
        for (int n = 0; n < 5; ++n) { b12[n] = cat8(lds_tr(lds + boff + n * 32), lds_tr(lds + boff + n * 32 + 1280)); PIN(b12[n]); }
        cw_add(lds, C_B12);
    };
    if (w8 < 4) {
        __builtin_amdgcn_s_setprio(3);
        if (w8 == 2) {
            cw_wait<8, 8>(lds, C_QFREE, 5);
            flag_wait<16>(ws, F_LN + 4 * (xcd >> 1) + (l & 3), get_tag(lds));
            const char* p = ws + WS_YF + (size_t)(xcd >> 1) * 655360 + bi * 20480;
            touch(p, l * 128, lds0 + L_DUMMY); touch(p + 8192, l * 128, lds0 + L_DUMMY); if (l < 32) touch(p + 16384, l * 128, lds0 + L_DUMMY);
            cw_set(lds, C_YRDY, 1);
        }
        {
            const char* ysrc = ws + WS_YF + (size_t)(panel * 4 + w8) * 40960;
            unsigned bias[3];
            u32x4 ya[8];
            auto first = [&]() {
#pragma unroll
                for (int t = 0; t < 3; ++t) asm volatile("global_load_ushort %0, %1, %2 offset:%3" : "=v"(bias[t]) : "v"((l & 31) * 2), "s"(a.b1 + hct * 192), "i"(t * 64) : "memory");
                static_for<0, 8>([&](auto I) { constexpr int i = I; gld128_dev<(i & 3) * 1024>(ya[i], ysrc + (i >> 2) * 4096, l16); });
            };
            bool stats_due = ct < 15 && w8 != 2;
#pragma unroll 1
            for (unsigned spins = 0; spins < (1u << 18); ++spins) {
                if (cw_ok<1>(lds, C_YRDY, 1)) break;
                if (stats_due && cw_ok<8>(lds, C_QFREE, 10)) { qkv_stats(w8, ct < 5, lds); stats_due = false; }
                __builtin_amdgcn_s_sleep(2);
            }
            CFENCE();
            first();
            if (stats_due) qkv_stats(w8, ct < 5, lds);
            else if (ct < 15) cw_wait<8, 4>(lds, C_QFREE, 10);
            __builtin_amdgcn_s_setprio(2);
            const unsigned boff = (unsigned)((8 * (g4 >> 1) + q4) * 192 + (g4 & 1) * 32 + p4 * 8);
            f32x16 hc[3];
            wait_vm<8>();
            const u32x2 one0 = {l < 32 ? 0x3f80u : 0u, 0u};
#pragma unroll
            for (int t = 0; t < 3; ++t) { PIN(bias[t]); hc[t] = MFMA8(((u32x2){bias[t], 0u}), one0, ((f32x16){})); }
#pragma unroll 1
            for (int st = 0; st < 5; ++st) {
                cw_wait<2, 1>(lds, C_WFULL, st + 1);
                if (st == 0) { STAMP(1); }
                lptr sb = lds + w1_slot(st) + boff;
                const char* ynext = ysrc + (st < 4 ? st + 1 : 4) * 8192;
                bf16x8 bw[2][3];
#pragma unroll
                for (int t = 0; t < 3; ++t) bw[0][t] = cat8(lds_tr(sb + t * 64), lds_tr(sb + t * 64 + 768));
                static_for<0, 8>([&](auto I) {
                    constexpr int i = I;
                    wait_vm<7>();
                    PIN(ya[i]);
#pragma unroll
                    for (int t = 0; t < 3; ++t) {
                        if (i < 7) bw[(i + 1) & 1][t] = cat8(lds_tr(sb + (i + 1) * 3072 + t * 64), lds_tr(sb + (i + 1) * 3072 + t * 64 + 768));
                        if (i == 6 && t == 2) cw_set(lds, C_WFREE + w8, st + 1);
                        SBAR();
                        hc[t] = MFMA32(bw[i & 1][t], __builtin_bit_cast(bf16x8, ya[i]), hc[t]); PIN(hc[t]);
                        SBAR();
                    }
                    gld128_dev_into<(i & 3) * 1024>(ya[i], ynext + (i >> 2) * 4096, l16);
                });
            }
            wait_vm<0>();
#pragma unroll
            for (int i = 0; i < 8; ++i) asm volatile("" :: "v"(ya[i]));
            STAMP(2);
            char* hdst = ws + WS_AF + ((size_t)(panel * 4 + w8) * NKS + hct * 6) * 1024;
#pragma unroll
            for (int t = 0; t < 3; ++t)
#pragma unroll
                for (int s = 0; s < 2; ++s) {
                    u32x4 v;
#pragma unroll
                    for (int e = 0; e < 4; ++e) v[e] = gelu2(hc[t][8 * s + 2 * e], hc[t][8 * s + 2 * e + 1]);
                    gst128_dev(hdst + (2 * t + s) * 1024, l16, v);
                }
            publish(lds, C_HDONE, 4, ws, F_H + panel * 16 + hct);
            STAMP(3);
        }
        const char* asrc = ws + WS_AF + ((size_t)(2 * rb + strip) * NKS + 4 * kq) * 1024;
        u32x4 an[3][4];
        cw_wait<1, 8>(lds, C_HRDY, 1);
        STAMP(4);
#pragma unroll
        for (int u = 0; u < 3; ++u) {
            const char* p = asrc + u * 8192;
            gld128_dev<0>(an[u][0], p, aoff); gld128_dev<256>(an[u][1], p, aoff); gld128_dev<2048>(an[u][2], p, aoff); gld128_dev<2304>(an[u][3], p, aoff);
        }
#pragma unroll
        for (int ti = 0; ti < 10; ++ti) acc[ti / 5][ti % 5] = (f32x4){};
#pragma unroll 1
        for (int c3 = 0; c3 < 12; c3 += 3) {
#pragma unroll
            for (int u = 0; u < 3; ++u) {
                const int c = c3 + u;
                cw_wait<2, 4>(lds, C_WFULL, 6 + c);
                wait_vm<8>();
#pragma unroll
                for (int i = 0; i < 4; ++i) PIN(an[u][i]);
                lptr sb = lds + (c & 3) * W2_STAGE + kq * (64 * 160) + b2off;
#pragma unroll
                for (int i = 0; i < 2; ++i) {
                    bf16x8 bw[5];
#pragma unroll
                    for (int n = 0; n < 5; ++n) bw[n] = cat8(lds_tr(sb + i * 5120 + n * 32), lds_tr(sb + i * 5120 + n * 32 + 1280));
                    if (i == 1) cw_set(lds, C_WFREE + w8, 6 + c);
#pragma unroll
                    for (int n = 0; n < 5; ++n) {
                        acc[0][n] = MFMA16(bw[n], __builtin_bit_cast(bf16x8, an[u][2 * i]), acc[0][n]); PIN(acc[0][n]); NOPS(PACE);
                        acc[1][n] = MFMA16(bw[n], __builtin_bit_cast(bf16x8, an[u][2 * i + 1]), acc[1][n]); PIN(acc[1][n]); NOPS(PACE);
                    }
                }
                SBAR();
                const char* p = asrc + (c + 3 < 12 ? c + 3 : 11) * 8192;
                gld128_dev_into<0>(an[u][0], p, aoff); gld128_dev_into<256>(an[u][1], p, aoff); gld128_dev_into<2048>(an[u][2], p, aoff); gld128_dev_into<2304>(an[u][3], p, aoff);
            }
        }
        wait_vm<0>();
#pragma unroll
        for (int i = 0; i < 12; ++i) asm volatile("" :: "v"(an[i >> 2][i & 3]));
    } else {
        const int hw = w8 - 4;
        __builtin_amdgcn_s_setprio(3);
        auto w1_touch = [&]() {
            const int ln = l < 60 ? l : 0, row = ln / 12;
            touch(a.w1 + (size_t)((bi * 4 + hw) * 5) * 3072 + (xcd & 1) * 1536, row * 3072 + (ln - row * 12) * 128, lds0 + L_DUMMY);
        };
        if (ct < 15) {
            const char* xa = a.x + (size_t)rp * 128 * 1280;
            const char* wb = a.wqkv + ct * 256;
            unsigned va[4], vb[4];
#pragma unroll
            for (int i = 0; i < 4; ++i) {
                va[i] = (unsigned)((32 * hw + 8 * i + (l >> 3)) * 1280 + (((l & 7) ^ (4 * (i & 1) + (l >> 4))) << 4));
                vb[i] = (unsigned)((16 * hw + 4 * i + (l >> 4)) * 3840 + (((l & 15) ^ ((l >> 4) << 2)) << 4));
            }
            auto stage = [&](int kt) {
                const unsigned dst = lds0 + (unsigned)hw * 4096u + (unsigned)(kt & 3) * 32768u;
                const char* pa = xa + kt * 128;
                const char* pb = wb + (size_t)kt * 64 * 3840;
#pragma unroll
                for (int i = 0; i < 4; ++i) { glds16<0, false>(pa, va[i], dst + i * 1024); glds16<0, false>(pb, vb[i], dst + 16384 + i * 1024); }
            };
#pragma unroll 1
            for (int kt = 0; kt < 10; ++kt) {
                if (kt >= 3) {
                    wait_vm<16>(); cw_set(lds, C_QFULL + hw, kt - 2);
                    if (!cw_ok<8>(lds, C_QFREE, kt - 3)) {
                        wait_vm<8>(); cw_set(lds, C_QFULL + hw, kt - 1);
                        cw_wait<8, HSLEEP>(lds, C_QFREE, kt - 3);
                    }
                    CFENCE();
                }
                stage(kt);
            }
        } else cw_wait<8, 8>(lds, C_QFREE, 10);
        w1_touch();
        wait_vm<17>(); cw_set(lds, C_QFULL + hw, 8);
        wait_vm<9>(); cw_set(lds, C_QFULL + hw, 9);
        wait_vm<1>(); cw_set(lds, C_QFULL + hw, 10);
        STAMP(1);
        const unsigned tag = get_tag(lds);
        if (hw < 2) {
            const int c0 = head * 20, ta = c0 >> 7, tb = (c0 + 19) >> 7;
            const int kvf = F_QKV + (5 + 5 * (l & 1) + ((l & 2) ? tb : ta)) * 16 + 8 * hw;
            const char* src = ws + WS_KV + (size_t)(head * 64 + hw * 32) * KVT;
            Polls pl;
            if (hw == 1) cw_wait<1, 1>(lds, C_KVGO, 1);
            else {
                cw_wait<1, 2>(lds, C_QDONE, 8);
                flag_wait_fast(pl, ws, (l & 8) ? F_QKV + ((l & 2) ? tb : ta) * 16 + 2 * qb + (l & 1) : kvf + 2 * (l & 4) + qb, tag);
                cw_set(lds, C_KVGO, 1);
            }
            STAMP(2);
            int buf = 0;
#pragma unroll 1
            for (int s = 0; s < 8; ++s) {
                if (s == 1) flag_wait<2>(ws, kvf + ((l >> 2) & 7), tag);
                if (s >= 3) cw_wait<4, 8>(lds, C_KVFREE + 4 * hw, s - 2);
                const unsigned dst = lds0 + (unsigned)(hw * KV_HALF + buf * KV_STAGE);
                const char* p = src + ((s + qb) & 7) * KV_STAGE;
                glds_4k(p, l16, dst); glds_4k(p + 4096, l16, dst + 4096);
                glds16<0, true>(p + 8192, l16, dst + 8192); glds16<1024, true>(p + 8192, l16, dst + 8192);
                if (l < 32) glds16<2048, true>(p + 8192, l16, dst + 8192);
                wait_vm<0>(); cw_set<4>(lds, C_KVFULL + 4 * hw + (l & 3), s + 1);
                buf = buf == 2 ? 0 : buf + 1;
                if (s == 2) {
                    const int u = bi * 2 + hw;
                    touch(a.w2 + u * 30720, l * 128, lds0 + L_DUMMY); touch(a.w2 + u * 30720 + 8192, l * 128, lds0 + L_DUMMY); touch(a.w2 + u * 30720 + 16384, l * 128, lds0 + L_DUMMY);
                    if (l < 48) touch(a.w2 + u * 30720 + 24576, l * 128, lds0 + L_DUMMY);
                    touch(a.wo + u * 12800, l * 128, lds0 + L_DUMMY); if (l < 36) touch(a.wo + u * 12800 + 8192, l * 128, lds0 + L_DUMMY);
                }
            }
            pl.release();
            if (b == 0 && hw == 0) {
#pragma unroll 1
                for (int i = 0; i < 4; ++i) flag_wait<16>(ws, F_H + 64 * i + l, tag);
                flag_set(ws, 0, tag);
            }
        } else {
            const int j = hw - 2;
            __builtin_amdgcn_s_setprio(2);
            unsigned vo[3];
#pragma unroll
            for (int i = 0; i < 3; ++i) { const int c = 64 * i + l, rho = c / 12; vo[i] = (unsigned)(rho * 3072 + (c - rho * 12) * 16); }
            const char* wsrc = a.w1 + (size_t)(64 * j) * 3072 + hct * 192;
            auto stw = [&](int st) {
                const unsigned dst = lds0 + (unsigned)w1_slot(st) + (unsigned)j * 12288u;
                const char* p = wsrc + (size_t)st * 128 * 3072;
#pragma unroll 1
                for (int h = 0; h < 4; ++h)
#pragma unroll
                    for (int i = 0; i < 3; ++i) glds16<0, false>(p + h * 16 * 3072, vo[i], dst + h * 3072 + i * 1024);
            };
#pragma unroll 1
            for (int st = 0; st < 5; ++st) {
                const int word = st < 3 ? C_QFREE + (l & 7) : C_WFREE + (l & 3); const unsigned need = st == 0 ? 0 : (st < 3 ? st + 6 : st - 2);
                if (!__all(cw_get(lds, word) >= need)) { wait_vm<0>(); cw_set(lds, C_WFULL + j, st); cw_wait<1, 2>(lds, word, need); }
                CFENCE();
                stw(st);
                if (st >= 2) { wait_vm<24>(); if (__builtin_amdgcn_readfirstlane(cw_get(lds, C_WFULL + j)) < 1u) cw_set(lds, C_WFULL + j, 1); wait_vm<12>(); cw_set(lds, C_WFULL + j, st); }
            }
            wait_vm<0>(); cw_set(lds, C_WFULL + j, 5);
            STAMP(2);
#pragma unroll 1
            for (int c = 0; c < 17; ++c) {
                if (c == 0 || c == 12 || c == 16) w2_offsets(c == 0 ? 0 : (c == 12 ? 1 : 2));
                if (c == 16 || !cw_ok<4>(lds, C_WFREE, c < 3 ? 5 : 2 + c)) {
                    wait_vm<0>(); cw_set(lds, C_WFULL + j, 5 + c);
                    if (c == 4 && j == 1) { flag_wait<8>(ws, F_H + (rb >> 1) * 16 + (l & 15), tag); cw_set(lds, C_HRDY, 1); }
                    if (c == 16) { load_b12(); cw_wait<1, 4>(lds, C_B12, 8); }
                    else cw_wait<4, 4>(lds, C_WFREE, c < 3 ? 5 : 2 + c);
                }
                CFENCE();
                stb(c);
                if (c >= 1) { wait_vm<10>(); cw_set(lds, C_WFULL + j, 5 + c); }
            }
        }
#pragma unroll
        for (int ti = 0; ti < 10; ++ti) acc[ti / 5][ti % 5] = (f32x4){};
    }
    if (w8 < 6) load_b12();
    u32x2 xi[3], bi2[3];
    unsigned eoff[3];
#pragma unroll
    for (int j = 0; j < 3; ++j) {
        const int u = (64 * j + l) < 160 ? 64 * j + l : l, row = u / 20, qd = u - row * 20;
        eoff[j] = (unsigned)(row * 1280 + qd * 8);
        gld64<0>(xi[j], a.x + (size_t)(rb * 64 + 8 * w8) * 1280 + nt * 160, eoff[j]);
        gld64<0>(bi2[j], a.b2 + nt * 160, qd * 8);
    }
    STAMP(5);
    Polls pl;
    wait_vm<0>();
    if (w8 == 4) {
        cw_wait<1, 4>(lds, C_ODONE, 1);
        flag_wait_fast(pl, ws, F_O + xcd * 32 + (l & 31), get_tag(lds));
    }
    LBAR();
    STAMP(6);
    const char* osrc = ws + WS_AF + ((size_t)(2 * rb + strip) * NKS + 96 + 2 * kq) * 1024;
    u32x4 on[5][2];
#pragma unroll
    for (int j = 0; j < 5; ++j) { gld128_dev<0>(on[j][0], osrc + j * 8192, aoff); gld128_dev<256>(on[j][1], osrc + j * 8192, aoff); }
    static_for<0, 5>([&](auto J) {
        constexpr int j = J;
        wait_vm<8 - 2 * j>();
        if (j == 0) pl.release();
        PIN(on[j][0]); PIN(on[j][1]);
        lptr sb = lds + (j & 3) * W2_STAGE + boff;
#pragma unroll
        for (int n = 0; n < 5; ++n) {
            const bf16x8 bw = j == 0 ? b12[n] : cat8(lds_tr(sb + n * 32), lds_tr(sb + n * 32 + 1280));
            acc[0][n] = MFMA16(bw, __builtin_bit_cast(bf16x8, on[j][0]), acc[0][n]);
            acc[1][n] = MFMA16(bw, __builtin_bit_cast(bf16x8, on[j][1]), acc[1][n]);
        }
        SBAR();
    });
    LBAR();
#pragma unroll
    for (int ti = 0; ti < 10; ++ti)
        *(LAS f32x4*)(lds + kq * 20480 + ((strip * 32 + (ti / 5) * 16 + r16) * 20 + (((ti % 5) * 4 + g4) ^ ((r16 >> 1) & 3))) * 16) = acc[ti / 5][ti % 5];
    LBAR();
#pragma unroll
    for (int j = 0; j < 3; ++j) {
        const int u = 64 * j + l, row = u / 20, qd = u - row * 20;
        if (u < 160) {
            PIN(xi[j]); PIN(bi2[j]);
            lptr p = lds + ((8 * w8 + row) * 20 + (qd ^ ((row >> 1) & 3))) * 16;
            const f32x4 v = (*(const LAS f32x4*)p + *(const LAS f32x4*)(p + 20480)) + (*(const LAS f32x4*)(p + 40960) + *(const LAS f32x4*)(p + 61440)) + (bf4_to_f32(xi[j]) + bf4_to_f32(bi2[j]));
            *(bf16x4*)(a.out + (size_t)(rb * 64 + 8 * w8) * 1280 + nt * 160 + eoff[j]) = __builtin_convertvector(v, bf16x4);
        }
    }
    STAMP(7);
    if (w8 < 4) { STAMP_FLUSH(2); } else { STAMP_FLUSH(3); }
}

extern "C" void kernel_launch(void* const* d_in, const int* in_sizes, int n_in, void* d_out, int out_size, void* d_ws, size_t ws_size, hipStream_t stream) {
    Args a;
    a.x = (const char*)d_in[0]; a.wqkv = (const char*)d_in[1]; a.wo = (const char*)d_in[2]; a.w1 = (const char*)d_in[4]; a.b1 = (const char*)d_in[5];
    a.w2 = (const char*)d_in[6]; a.b2 = (const char*)d_in[7]; a.ws = (char*)d_ws; a.out = (char*)d_out;
    k_all<<<256, 1024, 0, stream>>>(a);
}
```

```cpp
#include <hip/hip_runtime.h>
#include <hip/hip_bf16.h>
#include <stdint.h>
#include <type_traits>
#pragma clang diagnostic ignored "-Winline-asm"

typedef __bf16 bf16x8 __attribute__((ext_vector_type(8)));
typedef __bf16 bf16x4 __attribute__((ext_vector_type(4)));
typedef __bf16 bf16x2 __attribute__((ext_vector_type(2)));
typedef float f32x16 __attribute__((ext_vector_type(16)));
typedef float f32x8 __attribute__((ext_vector_type(8)));
typedef float f32x4 __attribute__((ext_vector_type(4)));
typedef float f32x2 __attribute__((ext_vector_type(2)));
typedef short s16x4 __attribute__((ext_vector_type(4)));
typedef unsigned u32x4 __attribute__((ext_vector_type(4)));
typedef unsigned u32x2 __attribute__((ext_vector_type(2)));
#define LAS __attribute__((address_space(3)))
#define DI __device__ __forceinline__
typedef LAS char* lptr;
typedef __attribute__((address_space(1))) unsigned gu32;

constexpr int QT = 1280;
constexpr int KVT = 2688;
constexpr int NKS = 136;
constexpr size_t WS_FLAG = 0;
constexpr size_t WS_Q = 131072;
constexpr size_t WS_KV = WS_Q + (size_t)32 * 64 * QT;
constexpr size_t WS_AF = WS_KV + (size_t)32 * 64 * KVT;
constexpr int F_QKV = 32;
constexpr int F_H = F_QKV + 240;
constexpr int F_O = F_H + 256;
constexpr float QSCALE = 0.22360679774997896f * 1.4426950408889634f;

constexpr int KV_STAGE = 4 * KVT, KV_HALF = 32768;
constexpr int W2_BASE = 65536, W2_STAGE = 128 * 160;
constexpr int WQ_BASE = 81920;
constexpr unsigned long long pack6(int a0, int a1, int a2, int a3, int a4, int a5, int a6, int a7, int a8, int a9) {
    const int v[10] = {a0, a1, a2, a3, a4, a5, a6, a7, a8, a9}; unsigned long long r = 0; for (int i = 0; i < 10; ++i) r |= (unsigned long long)v[i] << (6 * i); return r; }
constexpr unsigned long long X_TAB = pack6(0, 4, 8, 0, 4, 8, 0, 4, 12, 16), W1_TAB = pack6(32, 35, 12, 32, 35, 32, 35, 24, 28, 20);
DI int slot_of(unsigned long long tab, int k) { return (int)((tab >> (6 * k)) & 63) << 12; }
constexpr int CTL = 155648, L_FST = CTL + 512, L_PCS = CTL + 1536, L_FC1 = CTL + 2560, L_BIAS = CTL + 3072, L_DUMMY = CTL + 3584, L_FS2 = CTL + 4096, L_PST = WQ_BASE + 16384 + 12288;
enum { C_TAG = 0, C_QDONE, C_KVGO, C_HRDY, C_HDONE, C_ODONE, C_B12, C_FC1, C_XFULL = 8, C_XFREE = 12,
       C_KVFULL = 32  , C_KVFREE = 40, C_WFREE = 48, C_XCH = 52, C_WFULL = 56 };

constexpr int POLL_GAP = 4, KDELAY = 5, LATE_FROM = 3, KBASE = 2, POLL0 = 12, WPRIO = 0, WSL = 2, LSL = 6, A_PRIOQ = 3, M_PRIO = 3, W2_AFTER = 3, PACE = 5;

#define MFMA32(a, b, c) __builtin_amdgcn_mfma_f32_32x32x16_bf16((a), (b), (c), 0, 0, 0)
#define MFMA16(a, b, c) __builtin_amdgcn_mfma_f32_16x16x32_bf16((a), (b), (c), 0, 0, 0)
#define MFMA8(a, b, c) __builtin_amdgcn_mfma_f32_32x32x8bf16_1k(__builtin_bit_cast(s16x4, a), __builtin_bit_cast(s16x4, b), (c), 0, 0, 0)
#define SBAR() __builtin_amdgcn_sched_barrier(0)
#define CFENCE() asm volatile("" ::: "memory")
#define PIN(x) asm volatile("" : "+v"(x))
#define LBAR() do { CFENCE(); __builtin_amdgcn_s_waitcnt(0xC07F); __builtin_amdgcn_s_barrier(); CFENCE(); } while (0)
DI int lane_id() { unsigned z = 0; asm volatile("" : "+s"(z)); return __builtin_amdgcn_mbcnt_hi(~0u, __builtin_amdgcn_mbcnt_lo(~0u, z)); }
template <int N> DI void wait_vm() { asm volatile("s_waitcnt vmcnt(%0)" :: "i"(N) : "memory"); }
template <int I, int N, class F> DI void static_for(F&& f) { if constexpr (I < N) { f(std::integral_constant<int, I>{}); static_for<I + 1, N>(f); } }

template <int OFF, bool DEV>
DI void glds16(const void* sbase, unsigned voff, unsigned lds) {
    if constexpr (DEV) asm volatile("s_mov_b32 m0, %2\n\ts_nop 3\n\tglobal_load_lds_dwordx4 %0, %1 offset:%3 sc1" :: "v"(voff), "s"(sbase), "s"(lds), "i"(OFF) : "memory", "m0");
    else               asm volatile("s_mov_b32 m0, %2\n\ts_nop 3\n\tglobal_load_lds_dwordx4 %0, %1 offset:%3" :: "v"(voff), "s"(sbase), "s"(lds), "i"(OFF) : "memory", "m0");
}
DI void glds_4k(const void* sbase, unsigned voff, unsigned lds) {
    asm volatile("s_mov_b32 m0, %2\n\ts_nop 3\n\tglobal_load_lds_dwordx4 %0, %1 sc1\n\tglobal_load_lds_dwordx4 %0, %1 offset:1024 sc1\n\t"
                 "global_load_lds_dwordx4 %0, %1 offset:2048 sc1\n\tglobal_load_lds_dwordx4 %0, %1 offset:3072 sc1" :: "v"(voff), "s"(sbase), "s"(lds) : "memory", "m0");
}
DI void touch(const void* sbase, unsigned voff, unsigned lds) {
    asm volatile("s_mov_b32 m0, %2\n\ts_nop 3\n\tglobal_load_lds_dword %0, %1 sc1" :: "v"(voff), "s"(sbase), "s"(lds) : "memory", "m0");
}
template <int OFF> DI void gld128_dev(u32x4& d, const void* sbase, unsigned voff) {
    asm volatile("global_load_dwordx4 %0, %1, %2 offset:%3 sc1" : "=v"(d) : "v"(voff), "s"(sbase), "i"(OFF) : "memory"); }
template <int OFF> DI void gld128_dev_into(u32x4& d, const void* sbase, unsigned voff) {
    asm volatile("global_load_dwordx4 %0, %1, %2 offset:%3 sc1" : "+v"(d) : "v"(voff), "s"(sbase), "i"(OFF) : "memory"); }
template <int OFF> DI void gld64(u32x2& d, const void* sbase, unsigned voff) {
    asm volatile("global_load_dwordx2 %0, %1, %2 offset:%3" : "=v"(d) : "v"(voff), "s"(sbase), "i"(OFF) : "memory"); }
template <int OFF> DI void gld64_dev(u32x2& d, const void* sbase, unsigned voff) {
    asm volatile("global_load_dwordx2 %0, %1, %2 offset:%3 sc1" : "=v"(d) : "v"(voff), "s"(sbase), "i"(OFF) : "memory"); }
DI void gst128_dev(void* sbase, unsigned voff, u32x4 v) {
    asm volatile("global_store_dwordx4 %0, %1, %2 sc1\n\ts_nop 1" :: "v"(voff), "v"(v), "s"(sbase) : "memory"); }
DI void gst64_dev(void* sbase, unsigned voff, u32x2 v) {
    asm volatile("global_store_dwordx2 %0, %1, %2 sc1\n\ts_nop 1" :: "v"(voff), "v"(v), "s"(sbase) : "memory"); }
DI void gst64_out(void* sbase, unsigned voff, u32x2 v) { asm volatile("global_store_dwordx2 %0, %1, %2" :: "v"(voff), "v"(v), "s"(sbase) : "memory"); }

DI LAS unsigned* cw(lptr lds, int i) { return (LAS unsigned*)(lds + CTL) + i; }
DI unsigned cw_get(lptr lds, int i) { return __hip_atomic_load(cw(lds, i), __ATOMIC_RELAXED, __HIP_MEMORY_SCOPE_WORKGROUP); }
template <int LANES = 1> DI void cw_set(lptr lds, int i, unsigned v) {
    asm volatile("s_mov_b64 exec, %2\n\tds_write_b32 %0, %1\n\ts_mov_b64 exec, -1" :: "v"((unsigned)(uintptr_t)cw(lds, i)), "v"(v), "i"((1 << LANES) - 1) : "memory");
}
DI unsigned cw_add(lptr lds, int i) {
    unsigned old;
    asm volatile("s_nop 11\n\ts_mov_b64 exec, 1\n\tds_add_rtn_u32 %0, %1, %2\n\ts_mov_b64 exec, -1\n\ts_waitcnt lgkmcnt(0)" : "=&v"(old) : "v"((unsigned)(uintptr_t)cw(lds, i)), "v"(1u) : "memory");
    return __builtin_amdgcn_readfirstlane(old);
}
template <int SLEEP> DI void cw_wait_at(unsigned addr, unsigned need) {
    unsigned v, left = 1u << 18;
    asm volatile("s_nop 11\n"
                 "1:\n\tds_read_b32 %0, %2\n\ts_waitcnt lgkmcnt(0)\n\tv_cmp_gt_u32 vcc, %3, %0\n\ts_cbranch_vccz 2f\n\t"
                 "s_sleep %4\n\ts_sub_u32 %1, %1, 1\n\ts_cbranch_scc0 1b\n2:"
                 : "=&v"(v), "+s"(left) : "v"(addr), "s"(need), "i"(SLEEP) : "memory", "vcc", "scc");
}
DI unsigned cw_addr(lptr lds, int i) { unsigned p = (unsigned)(uintptr_t)cw(lds, i); PIN(p); return p; }
template <int LANES> DI void cw_set_at(unsigned addr, unsigned v) {
    asm volatile("s_mov_b64 exec, %2\n\tds_write_b32 %0, %1\n\ts_mov_b64 exec, -1" :: "v"(addr), "v"(v), "i"((1 << LANES) - 1) : "memory");
}
template <int N, int SLEEP> DI void cw_wait(lptr lds, int i, unsigned need) {
    cw_wait_at<SLEEP>((unsigned)(uintptr_t)cw(lds, N > 1 ? i + (lane_id() & (N - 1)) : i), need);
}
DI void flag_set(char* ws, int idx, unsigned tag) {
    asm volatile("s_mov_b64 exec, 1\n\tglobal_store_dword %0, %1, %2 sc1\n\ts_mov_b64 exec, -1" :: "v"(idx * 128), "v"(tag), "s"(ws + WS_FLAG) : "memory");
}
template <int SLEEP> DI void flag_wait(char* ws, int idx  , unsigned tag) {
#pragma unroll 1
    for (unsigned spins = 0; spins < (1u << 17); ++spins) {
        if (__all(__hip_atomic_load((gu32*)(ws + WS_FLAG) + idx * 32, __ATOMIC_RELAXED, __HIP_MEMORY_SCOPE_AGENT) == tag)) break;
        __builtin_amdgcn_s_sleep(SLEEP);
    }
    CFENCE();
}
struct Polls { unsigned v0 = 0, v1 = 0, v2 = 0; DI void release() { asm volatile("" :: "v"(v0), "v"(v1), "v"(v2)); } };
DI void flag_wait_fast(Polls& pl, char* ws, int idx  , unsigned tag) {
    unsigned &v0 = pl.v0, &v1 = pl.v1, &v2 = pl.v2;
    const char* base = ws + WS_FLAG; const unsigned off = idx * 128;
#define POLL(v) asm volatile("global_load_dword %0, %1, %2 sc1" : "=v"(v) : "v"(off), "s"(base) : "memory")
#define SEEN(v) asm volatile("s_waitcnt vmcnt(2)" : "+v"(v) :: "memory"); if (__all(v == tag)) break; __builtin_amdgcn_s_sleep(POLL_GAP); POLL(v)
    wait_vm<0>();
    POLL(v0); __builtin_amdgcn_s_sleep(POLL_GAP); POLL(v1); __builtin_amdgcn_s_sleep(POLL_GAP); POLL(v2);
#pragma unroll 1
    for (unsigned spins = 0; spins < (1u << 16); ++spins) { SEEN(v0); SEEN(v1); SEEN(v2); }
    CFENCE();
#undef POLL
#undef SEEN
}
DI unsigned get_tag(lptr lds) { return __builtin_amdgcn_readfirstlane(cw_get(lds, C_TAG)); }
DI void publish(lptr lds, int ctr, unsigned n, char* ws, int flag) {
    unsigned tag = cw_get(lds, C_TAG), fo = flag; PIN(tag); PIN(fo);
    wait_vm<0>();
    if (cw_add(lds, ctr) == n - 1) flag_set(ws, fo, tag);
}

DI f32x4 bf4_to_f32(u32x2 v) { return (f32x4){__uint_as_float(v.x << 16), __uint_as_float(v.x & 0xffff0000u), __uint_as_float(v.y << 16), __uint_as_float(v.y & 0xffff0000u)}; }
DI bf16x8 lds_b128(lptr p) { return *(const LAS bf16x8*)p; }
DI bf16x4 lds_b64(lptr p) { return *(const LAS bf16x4*)p; }
DI bf16x4 lds_tr(lptr p) { return __builtin_bit_cast(bf16x4, __builtin_amdgcn_ds_read_tr16_b64_v4i16((LAS s16x4*)p)); }
DI bf16x8 cat8(bf16x4 a, bf16x4 b) { return __builtin_shufflevector(a, b, 0, 1, 2, 3, 4, 5, 6, 7); }
DI f32x2 both_halves(float v) {
    auto r = __builtin_amdgcn_permlane32_swap(__float_as_uint(v), __float_as_uint(v), false, false);
    return (f32x2){__uint_as_float(r[0]), __uint_as_float(r[1])};
}
DI float max_halves(float v) { const f32x2 t = both_halves(v); return fmaxf(t.x, t.y); }
DI float opq(float v) { asm("" : "+v"(v)); return v; }
DI unsigned gelu2(float a, float b) {
    const f32x2 x = {a, b};
    const f32x2 z = x * __builtin_elementwise_fma(x * x, (f32x2){-0.044715f * 2.302208198f, -0.044715f * 2.302208198f}, (f32x2){-2.302208198f, -2.302208198f});
    const f32x2 d = (f32x2){__builtin_amdgcn_exp2f(z.x), __builtin_amdgcn_exp2f(z.y)} + 1.0f;
    const f32x2 y = x * (f32x2){__builtin_amdgcn_rcpf(d.x), __builtin_amdgcn_rcpf(d.y)};
    return __builtin_bit_cast(unsigned, __builtin_convertvector(y, bf16x2));
}

struct Args { const char *x, *wqkv, *wo, *w1, *b1, *w2, *b2; char *ws, *out; };

DI void kv_delay(int rp) {
#pragma unroll 1
    for (int i = (rp & 7) ? (rp & 7) + KBASE : 0; i > 0; --i) __builtin_amdgcn_s_sleep(KDELAY);
}
template <bool ISV>
DI void qkv_tile(int w, int rp, int ct, char* __restrict__ ws, lptr lds) {
    const int l = lane_id(), r = l & 31, hh = l >> 5, wr = w & 1, wc = w >> 1;
    const int g4 = l >> 4, q4 = (l & 15) >> 2, p4 = l & 3;
    const int rot = wc ^ (2 * wr);
    unsigned aoff[4], boff[4];
#pragma unroll
    for (int i = 0; i < 4; ++i) {
        aoff[i] = (unsigned)((64 * wr + r) * 128 + (((2 * (i ^ rot) + hh) ^ ((r >> 1) & 7)) << 4));
        boff[i] = (unsigned)(WQ_BASE + (i ^ rot) * 4096 + (8 * (g4 >> 1) + q4) * 256 + (((4 * wc + 2 * (g4 & 1) + (p4 >> 1)) ^ (q4 << 2)) << 4) + (p4 & 1) * 8);
    }
    float sx[2] = {0.f, 0.f}, sq[2] = {0.f, 0.f};
    auto stats = [&](unsigned u, float& s, float& q) {
        const float lo = __uint_as_float(u << 16), hi = __uint_as_float(u & 0xffff0000u);
        s += lo; q = __builtin_fmaf(lo, lo, q); s += hi; q = __builtin_fmaf(hi, hi, q);
        PIN(s); PIN(q);
    };
    f32x16 acc[2] = {};
    f32x4 csum = {};
    const unsigned selw = ((l & 15) == (g4 & 1)) ? 0x3f803f80u : 0u;
    const bf16x8 sel = __builtin_bit_cast(bf16x8, (u32x4){selw, selw, selw, selw});
    bf16x8 a0[4], a1[4], b[4];
    auto rdb = [&](int i) { b[i] = cat8(lds_tr(lds + boff[i]), lds_tr(lds + boff[i] + 1024)); };
    auto rda = [&](int i) { a0[i] = lds_b128(lds + aoff[i]); a1[i] = lds_b128(lds + aoff[i] + 4096); };
    auto mm0 = [&](int i) { acc[0] = ISV ? MFMA32(a0[i], b[i], acc[0]) : MFMA32(b[i], a0[i], acc[0]); PIN(acc[0]); };
    auto mm1 = [&](int i) { acc[1] = ISV ? MFMA32(a1[i], b[i], acc[1]) : MFMA32(b[i], a1[i], acc[1]); PIN(acc[1]); };
    LBAR();
    rdb(0); rda(0); rdb(1); rda(1);
    int slot = 0;
#pragma unroll 1
    for (int kt = 0; kt < 10; ++kt) {
        const unsigned xstep = slot_of(X_TAB, kt + 1) - slot_of(X_TAB, kt), wstep = slot == 2 ? -32768 : 16384; slot = slot == 2 ? 0 : slot + 1;
        const u32x4 s0 = __builtin_bit_cast(u32x4, a0[0]), s1 = __builtin_bit_cast(u32x4, a1[0]);
        SBAR();
        mm0(0); rdb(2); stats(s0[0], sx[0], sq[0]); SBAR();
        mm1(0); rda(2); stats(s0[1], sx[0], sq[0]); SBAR();
        mm0(1); rdb(3); stats(s0[2], sx[0], sq[0]); SBAR();
        mm1(1); rda(3); stats(s0[3], sx[0], sq[0]); SBAR();
        csum = MFMA16(sel, b[0], csum); csum = MFMA16(sel, b[1], csum); PIN(csum);
#pragma unroll
        for (int i = 0; i < 4; ++i) { aoff[i] += xstep; boff[i] += wstep; }
        SBAR();
        LBAR();
        mm0(2); rdb(0); stats(s1[0], sx[1], sq[1]); SBAR();
        mm1(2); rda(0); stats(s1[1], sx[1], sq[1]); SBAR();
        mm0(3); rdb(1); stats(s1[2], sx[1], sq[1]); SBAR();
        mm1(3); rda(1); stats(s1[3], sx[1], sq[1]); SBAR();
    }
#pragma unroll
    for (int mt = 0; mt < 2; ++mt) {
        const f32x2 ts = both_halves(sx[mt]), tq = both_halves(sq[mt]);
        *(LAS f32x2*)(lds + L_PST + (wc * 128 + 64 * wr + 32 * mt + r) * 8) = (f32x2){ts.x + opq(ts.y), tq.x + opq(tq.y)};
    }
    if (l < 16) { *(LAS float*)(lds + L_PCS + (wr * 128 + 32 * wc + l) * 4) = csum[0]; *(LAS float*)(lds + L_PCS + (wr * 128 + 32 * wc + 16 + l) * 4) = csum[1]; }
    __builtin_amdgcn_s_setprio(3);
    LBAR();
    const LAS f32x2* fst = (const LAS f32x2*)(lds + L_FST);
    const LAS float* fcs = (const LAS float*)(lds + L_PCS);
    const int tt0 = rp * 4 + 2 * wr;
    if (ISV) {
        const int cc = (ct - 10) * 128 + 32 * wc + r, head = cc / 20, d = cc - head * 20;
        float c = fcs[32 * wc + r] + fcs[128 + 32 * wc + r];
        unsigned off = (unsigned)(head * 64 * KVT + QT + (hh * 20 + d) * 16);
        char* dst = ws + WS_KV + (size_t)tt0 * KVT;
        PIN(c); PIN(off);
        if (d == 19 && ct < 15) {
            const unsigned o_ = hh ? 0u : 0x3f803f80u;
            gst128_dev(dst, off + 1280 - 304 - hh * 304, (u32x4){o_, o_, o_, o_}); gst128_dev(dst + KVT, off + 1280 - 304 - hh * 304, (u32x4){o_, o_, o_, o_});
        }
        LBAR();
        if (ct == 15) return;
        f32x4 ab[2][2][4];
#pragma unroll
        for (int mt = 0; mt < 2; ++mt)
#pragma unroll
            for (int s = 0; s < 2; ++s)
#pragma unroll
                for (int j = 0; j < 4; ++j) ab[mt][s][j] = *(const LAS f32x4*)(fst + 64 * wr + 32 * mt + 16 * s + 8 * (j >> 1) + 4 * hh + 2 * (j & 1));
        SBAR();
        kv_delay(rp);
#pragma unroll
        for (int mt = 0; mt < 2; ++mt)
#pragma unroll
            for (int s = 0; s < 2; ++s) {
                f32x8 v;
#pragma unroll
                for (int j = 0; j < 8; ++j) v[j] = opq(__builtin_fmaf(ab[mt][s][j >> 1][2 * (j & 1)], acc[mt][8 * s + j], opq(ab[mt][s][j >> 1][2 * (j & 1) + 1] * c)));
                gst128_dev(dst + mt * KVT + s * 640, off, __builtin_bit_cast(u32x4, __builtin_convertvector(v, bf16x8)));
            }
    } else {
        const bool isq = ct < 5;
        const int tile = isq ? QT : KVT;
        char* dst = ws + (isq ? WS_Q : WS_KV) + (size_t)tt0 * tile;
        f32x4 c4[4]; unsigned off[4]; int gs = -1;
#pragma unroll
        for (int g = 0; g < 4; ++g) {
            c4[g] = *(const LAS f32x4*)(fcs + 32 * wc + 8 * g + 4 * hh) + *(const LAS f32x4*)(fcs + 128 + 32 * wc + 8 * g + 4 * hh);
            const int pi = (isq ? ct : ct - 5) * 16 + 4 * wc + g, k2 = pi / 5, e = pi - 5 * k2;
            off[g] = e == 2 ? (unsigned)((2 * k2 + hh) * 64 * tile + 1024 + r * 8) : (unsigned)((2 * k2 + (e > 2)) * 64 * tile + (e > 2 ? e - 3 : e) * 512 + hh * tile + r * 16);
            if (e == 2) gs = g;
            PIN(c4[g]); PIN(off[g]);
        }
        LBAR();
        f32x2 ab[2]; float aby[2];
#pragma unroll
        for (int mt = 0; mt < 2; ++mt) { ab[mt] = fst[64 * wr + 32 * mt + r]; aby[mt] = opq(ab[mt].y); }
        u32x2 pk[4][2];
#pragma unroll
        for (int g = 0; g < 4; ++g)
#pragma unroll
            for (int mt = 0; mt < 2; ++mt) {
                f32x4 v;
#pragma unroll
                for (int j = 0; j < 4; ++j) v[j] = __builtin_fmaf(ab[mt].x, acc[mt][4 * g + j], aby[mt] * c4[g][j]);
                pk[g][mt] = __builtin_bit_cast(u32x2, __builtin_convertvector(v, bf16x4)); PIN(pk[g][mt]);
            }
        if (!isq) kv_delay(rp);
#pragma unroll
        for (int g = 0; g < 4; ++g) {
            if (g == gs) { gst64_dev(dst, off[g], pk[g][0]); gst64_dev(dst + tile, off[g], pk[g][1]); }
            else {
                const auto sx = __builtin_amdgcn_permlane32_swap(pk[g][0].x, pk[g][1].x, false, false), sy = __builtin_amdgcn_permlane32_swap(pk[g][0].y, pk[g][1].y, false, false);
                gst128_dev(dst, off[g], (u32x4){sx[0], sy[0], sx[1], sy[1]});
            }
        }
    }
    if (ct < 5 || (rp & 7) < LATE_FROM) publish(lds, C_QDONE, 8, ws, F_QKV + ct * 16 + rp);
}

DI void h_tile(int w, int rp, int ct, const char* __restrict__ b1, char* __restrict__ ws, lptr lds) {
    const int mw = w - 8;
    const int l = lane_id(), r = l & 31, hh = l >> 5, g4 = l >> 4, q4 = (l & 15) >> 2, p4 = l & 3;
    unsigned xoff = (unsigned)((32 * mw + r) * 128 + ((hh ^ ((r >> 1) & 7)) << 4)), woff[3];
    int tt[3];
#pragma unroll
    for (int t = 0; t < 3; ++t) { tt[t] = (t + mw) % 3; woff[t] = (unsigned)(131072 + (8 * (g4 >> 1) + q4) * 192 + (g4 & 1) * 32 + p4 * 8 + tt[t] * 64); }
    f32x16 hc[3] = {};
    f32x4 csum = {};
    const unsigned selw = ((l & 15) == (g4 & 1)) ? 0x3f803f80u : 0u;
    const bf16x8 sel = __builtin_bit_cast(bf16x8, (u32x4){selw, selw, selw, selw});
    bf16x8 xf[4], bw[4][3];
    auto rdw = [&](int ks, int t) { bw[ks][t] = cat8(lds_tr(lds + woff[t] + ks * 3072), lds_tr(lds + woff[t] + ks * 3072 + 768)); };
    auto rdx = [&](int ks) { xf[ks] = lds_b128(lds + (xoff ^ (unsigned)(ks << 5))); };
    unsigned early;
    asm volatile("global_load_dword %0, %1, %2 sc1" : "=v"(early) : "v"(mw == 0 ? 0 : (l < 48 ? l * 4 : 0)), "s"(mw == 0 ? (const char*)ws + WS_FLAG : b1 + ct * 192) : "memory");
    LBAR();
    rdx(0); rdw(0, 0); rdw(0, 1); rdw(0, 2); rdx(1); rdw(1, 0); rdw(1, 1); rdw(1, 2);
    wait_vm<0>(); PIN(early);
    if (mw == 0) *cw(lds, l) = l == C_TAG ? early + 1u : 0u;
    if (mw == 1 && l < 48) *(LAS f32x2*)(lds + L_BIAS + l * 8) = (f32x2){__uint_as_float(early << 16), __uint_as_float(early & 0xffff0000u)};
    auto half = [&](int k0) {
#pragma unroll
        for (int ks = k0; ks < k0 + 2; ++ks) {
            const int nx = (ks + 2) & 3;
            SBAR();
            csum = MFMA16(sel, bw[ks][0], csum); PIN(csum); SBAR();
            hc[0] = MFMA32(bw[ks][0], xf[ks], hc[0]); PIN(hc[0]); rdx(nx); rdw(nx, 0); SBAR();
            hc[1] = MFMA32(bw[ks][1], xf[ks], hc[1]); PIN(hc[1]); rdw(nx, 1); SBAR();
            hc[2] = MFMA32(bw[ks][2], xf[ks], hc[2]); PIN(hc[2]); rdw(nx, 2); SBAR();
        }
    };
    auto next = [&](int kt) {
        const unsigned xstep = slot_of(X_TAB, kt + 1) - slot_of(X_TAB, kt), wstep = slot_of(W1_TAB, kt + 1) - slot_of(W1_TAB, kt);
        xoff += xstep; woff[0] += wstep; woff[1] += wstep; woff[2] += wstep; SBAR();
    };
    half(0); next(0);
#pragma unroll 1
    for (int kt = 1; kt < 5; ++kt) { LBAR(); half(2); half(0); next(kt); }
    __builtin_amdgcn_s_setprio(2);
#pragma unroll 1
    for (int k = 5; k <= 12; ++k) LBAR();
    const int fword = C_XFULL + (l & 3);
#pragma unroll 1
    for (int kt = 5; kt < 10; ++kt) {
        half(2);
        const unsigned full = cw_get(lds, fword);
        half(0); next(kt);
        cw_set(lds, C_XFREE + mw, kt + 1);
        const unsigned need = kt == 5 ? 0 : (kt < 9 ? kt + 2 : 10);
        if (__builtin_expect(!__all(full >= need), 0)) cw_wait<1, 1>(lds, fword, need);
        CFENCE();
    }
    half(2);
    if (l < 16) { *(LAS float*)(lds + L_FC1 + (32 * tt[0] + l) * 4) = csum[0]; *(LAS float*)(lds + L_FC1 + (32 * tt[0] + 16 + l) * 4) = csum[1]; }
    cw_add(lds, C_FC1);
    cw_wait<1, 1>(lds, C_FC1, 4);
    const f32x2 ab = *(const LAS f32x2*)(lds + L_FS2 + (32 * mw + r) * 8);
    const float a2 = ab.x, nb = opq(ab.y);
    char* hdst = ws + WS_AF + ((size_t)(rp * 4 + mw) * NKS + ct * 6) * 1024;
    u32x4 hv[3][2];
#pragma unroll
    for (int t = 0; t < 3; ++t)
#pragma unroll
        for (int s = 0; s < 2; ++s) {
            u32x4& v = hv[t][s];
#pragma unroll
            for (int q = 0; q < 2; ++q) {
                const int f = (32 * tt[t] + 16 * s + 8 * q + 4 * hh) * 4;
                const f32x4 c = *(const LAS f32x4*)(lds + L_FC1 + f) * nb + *(const LAS f32x4*)(lds + L_BIAS + f);
#pragma unroll
                for (int e = 0; e < 2; ++e)
                    v[2 * q + e] = gelu2(__builtin_fmaf(a2, hc[t][8 * s + 4 * q + 2 * e], c[2 * e]), __builtin_fmaf(a2, hc[t][8 * s + 4 * q + 2 * e + 1], c[2 * e + 1]));
            }
            PIN(v);
        }
    cw_wait<1, 4>(lds, C_KVFULL, 3);
#pragma unroll
    for (int t = 0; t < 3; ++t)
#pragma unroll
        for (int s = 0; s < 2; ++s) gst128_dev(hdst + (2 * tt[t] + s) * 1024, l * 16, hv[t][s]);
    publish(lds, C_HDONE, 4, ws, F_H + rp * 16 + ct);
}

DI float hmax16(const f32x16& s) {
    float a = fmaxf(fmaxf(s[0], s[1]), s[2]), b = fmaxf(fmaxf(s[3], s[4]), s[5]);
    a = fmaxf(fmaxf(a, s[6]), s[7]); b = fmaxf(fmaxf(b, s[8]), s[9]);
    a = fmaxf(fmaxf(a, s[10]), s[11]); b = fmaxf(fmaxf(b, s[12]), s[13]);
    return fmaxf(fmaxf(a, b), fmaxf(s[14], s[15]));
}
DI void set_ref(bf16x4& q2, float m, int hh) {
    const float nm = -m;
    const __bf16 a = (__bf16)nm; const float r1 = nm - (float)a;
    const __bf16 b = (__bf16)r1; const __bf16 c = (__bf16)(r1 - (float)b);
    const u32x2 n = __builtin_bit_cast(u32x2, (bf16x4){a, b, c, (__bf16)0.f}), o = __builtin_bit_cast(u32x2, q2);
    q2 = __builtin_bit_cast(bf16x4, (u32x2){hh ? n.x : o.x, hh ? n.y : o.y});
}

DI void attention(int w, int head, int qb, int late  , char* __restrict__ ws, lptr lds) {
    const int g = w & 3, kh = w >> 2;
    const char* kvh = ws + WS_KV + (size_t)head * 64 * KVT;
    const f32x16 zero = {};
    bf16x8 q1[2]; bf16x4 q2[2];
    f32x16 o[2] = {};
    {
        const int l = lane_id(), r = l & 31, hh = l >> 5;
        const unsigned k2rel = hh ? 2560 : 1024 + r * 8;
        unsigned k1off = (unsigned)(kh * KV_HALF + l * 16);
        unsigned k2off = (unsigned)(kh * KV_HALF) + k2rel;
        unsigned v0off = (unsigned)(kh * KV_HALF + (r < 20 ? QT + (hh * 20 + r) * 16 : (r == 20 ? 2560 : 2576)));
        unsigned v1off = v0off + (r < 20 ? 640 : 0);
        const char* qp = ws + WS_Q + (size_t)(head * 64 + qb * 8 + 2 * g) * QT;
        const char* kp = kvh;
        unsigned l16 = l * 16, r8 = r * 8, k2r_off = k2rel;
        PIN(k1off); PIN(k2off); PIN(v0off); PIN(v1off); PIN(l16); PIN(r8); PIN(k2r_off); PIN(o[0]); PIN(o[1]);
        asm volatile("" : "+s"(qp), "+s"(kp));
        __builtin_amdgcn_s_setprio(WPRIO);
        cw_wait<1, WSL>(lds, C_KVGO, 1);
        __builtin_amdgcn_s_setprio(3);
        u32x4 q1r[2], k1r; u32x2 q2r[2], k2r;
        gld128_dev<0>(k1r, kp, l16); gld64_dev<0>(k2r, kp, k2r_off);
        gld128_dev<0>(q1r[0], qp, l16); gld128_dev<QT>(q1r[1], qp, l16);
        gld64_dev<1024>(q2r[0], qp, r8); gld64_dev<1024 + QT>(q2r[1], qp, r8);
        wait_vm<0>();
        PIN(q1r[0]); PIN(q1r[1]); PIN(q2r[0]); PIN(q2r[1]); PIN(k1r); PIN(k2r);
        f32x16 sa, sb;
#pragma unroll
        for (int i = 0; i < 2; ++i) {
            q1[i] = __builtin_bit_cast(bf16x8, q1r[i]);
            q2[i] = __builtin_bit_cast(bf16x4, (u32x2){hh ? 0u : q2r[i].x, hh ? 0u : q2r[i].y});
        }
        sa = MFMA32(__builtin_bit_cast(bf16x8, k1r), q1[0], zero); sb = MFMA32(__builtin_bit_cast(bf16x8, k1r), q1[1], zero);
        sa = MFMA8(k2r, q2[0], sa); sb = MFMA8(k2r, q2[1], sb);
        PIN(sa); PIN(sb);
        if (late) publish(lds, C_QDONE, 8, ws, late);
        cw_wait<1, 0>(lds, C_KVFULL + w, 1);
        bf16x8 k1 = lds_b128(lds + k1off); bf16x4 k2 = lds_b64(lds + k2off);
        bf16x8 v0 = lds_b128(lds + v0off), v1 = lds_b128(lds + v1off);
        SBAR();
        set_ref(q2[0], max_halves(hmax16(sa)), hh); set_ref(q2[1], max_halves(hmax16(sb)), hh);
        if (kh) __builtin_amdgcn_s_setprio(0); else __builtin_amdgcn_s_setprio(1);
        bf16x8 p1 = {};
        float ta = 0.f, tb = 0.f;
        sa = MFMA8(k2, q2[0], zero); sa = MFMA32(k1, q1[0], sa);
        sb = MFMA8(k2, q2[1], zero);
#define EX(x) __builtin_amdgcn_exp2f(x)
#define CV(P, i, a, b) do { const f32x2 t_ = {a, b}; const bf16x2 c_ = __builtin_convertvector(t_, bf16x2); P[2 * (i)] = c_[0]; P[2 * (i) + 1] = c_[1]; } while (0)
#define GRP(SC, j, PA, ia, PB, ib) do { float e0_ = EX(SC[j]), e1_ = EX(SC[j + 1]); PIN(e0_); PIN(e1_); CV(PA, ia, ta, tb); PIN(PA); SBAR(); \
        ta = EX(SC[j + 2]); tb = EX(SC[j + 3]); PIN(ta); PIN(tb); CV(PB, ib, e0_, e1_); PIN(PB); SBAR(); } while (0)
#define UNIT(SC, SN, IC, IN, AFTER_D, AFTER_B, AFTER_C) do { bf16x8 p0; \
        GRP(SC, 0, p1, 3, p0, 0);  o[IN] = MFMA32(v1, p1, o[IN]); PIN(o[IN]); AFTER_D; SBAR(); \
        GRP(SC, 4, p0, 1, p0, 2);  SN = MFMA32(k1, q1[IN], SN); PIN(SN); AFTER_B; SBAR(); \
        GRP(SC, 8, p0, 3, p1, 0);  o[IC] = MFMA32(v0, p0, o[IC]); PIN(o[IC]); AFTER_C; SBAR(); \
        GRP(SC, 12, p1, 1, p1, 2); SC = MFMA8(k2, q2[IC], zero); PIN(SC); SBAR(); \
        } while (0)
        static_assert(C_KVFREE == C_KVFULL + 8, "layout");
        const unsigned sync = (unsigned)(uintptr_t)cw(lds, l == 0 ? C_KVFULL + w : C_KVFREE + (w ^ 4));
        const int lead = 1 - kh;
        int buf = 0;
#pragma unroll 1
        for (int s = 0; s < 8; ++s) {
            const int nbuf = buf == 2 ? 0 : buf + 1;
            lptr cb = lds + buf * KV_STAGE;
            lptr nb = lds + nbuf * KV_STAGE;
            unsigned full = 0;
#pragma unroll
            for (int j = 0; j < 4; ++j) {
                lptr cur = cb + j * KVT;
                lptr nxt = j == 3 ? nb : cb + (j + 1) * KVT;
#define REL() do { if (j == 3) { asm volatile("s_mov_b64 exec, 1\n\tds_write_b32 %0, %1 offset:32\n\ts_mov_b64 exec, -1" :: "v"(sync), "v"(s + 1) : "memory"); \
                    full = __hip_atomic_load((LAS unsigned*)(uintptr_t)sync, __ATOMIC_RELAXED, __HIP_MEMORY_SCOPE_WORKGROUP); } } while (0)
#define ACQ() do { if (j == 3) { if (__builtin_expect(__builtin_amdgcn_readfirstlane(full) < (unsigned)(s + 2), 0)) cw_wait<1, 1>(lds, C_KVFULL + w, s + 2); \
                    asm volatile("s_setprio 0\n\ts_cmp_gt_i32 %0, %1\n\ts_cbranch_scc0 1f\n\ts_setprio 1\n1:" :: "s"((int)__builtin_amdgcn_readlane(full, 1)), "s"(s - lead) : "scc"); CFENCE(); } } while (0)
                UNIT(sa, sb, 0, 1, v1 = lds_b128(cur + v1off); REL(), ACQ(); k2 = lds_b64(nxt + k2off); k1 = lds_b128(nxt + k1off), (void)0);
                UNIT(sb, sa, 1, 0, (void)0, (void)0, v0 = lds_b128(nxt + v0off));
#undef REL
#undef ACQ
            }
            buf = nbuf;
        }
        CV(p1, 3, ta, tb);
        o[1] = MFMA32(v1, p1, o[1]);
#undef UNIT
#undef GRP
    }
    __builtin_amdgcn_s_setprio(3);
    const int l = lane_id(), r = l & 31, hh = l >> 5;
    if (kh == 0) {
        cw_wait<4, 3>(lds, C_KVFREE, 8);
#pragma unroll
        for (int i = 0; i < 2; ++i)
#pragma unroll
            for (int q = 0; q < 3; ++q)
                *(LAS f32x4*)(lds + ((g * 2 + i) * 3 + q) * 1024 + l * 16) = (f32x4){o[i][4 * q], o[i][4 * q + 1], o[i][4 * q + 2], o[i][4 * q + 3]};
        cw_set(lds, C_XCH + g, 1);
        return;
    }
    char* af = ws + WS_AF + ((size_t)(qb * 8 + 2 * g) * NKS + 96) * 1024;
    auto put = [&](int i, const f32x4 (&res)[3]) {
        const float inv = __builtin_amdgcn_rcpf(opq(both_halves(res[2][0]).y));
        char* p = af + i * (NKS * 1024);
        gst128_dev(p + head * 1024, l * 16, __builtin_bit_cast(u32x4, cat8(__builtin_convertvector(res[0] * inv, bf16x4), __builtin_convertvector(res[1] * inv, bf16x4))));
        if (!hh) gst64_dev(p + (32 + (head >> 2)) * 1024 + ((head >> 1) & 1) * 512 + (head & 1) * 8, r * 16, __builtin_bit_cast(u32x2, __builtin_convertvector(res[2] * inv, bf16x4)));
    };
    cw_wait<1, 1>(lds, C_XCH + g, 1);
    f32x4 res[2][3];
#pragma unroll
    for (int i = 0; i < 2; ++i)
#pragma unroll
        for (int q = 0; q < 3; ++q)
            res[i][q] = *(const LAS f32x4*)(lds + ((g * 2 + i) * 3 + q) * 1024 + l * 16) + (f32x4){o[i][4 * q], o[i][4 * q + 1], o[i][4 * q + 2], o[i][4 * q + 3]};
    put(0, res[0]); put(1, res[1]);
    float chk = 0.f;
#pragma unroll
    for (int i = 0; i < 2; ++i)
#pragma unroll
        for (int q = 0; q < 3; ++q)
#pragma unroll
            for (int e = 0; e < 4; ++e) chk = opq(chk + fabsf(res[i][q][e]));
    const bool low = hh && !(fminf(res[0][2][0], res[1][2][0]) > 0.25f);
    if (__builtin_expect(__any(!(chk < 1.0e37f) || low), 0)) {
        const unsigned k2rel = hh ? 2560 : 1024 + r * 8;
        const unsigned vrel = r < 20 ? QT + (hh * 20 + r) * 16 : (r == 20 ? 2560 : 2576);
#pragma unroll 1
        for (int i = 0; i < 2; ++i) {
            const bf16x8 qa = i ? q1[1] : q1[0];
            bf16x4 qz = i ? q2[1] : q2[0]; set_ref(qz, 0.f, hh);
            float m = -INFINITY;
#pragma unroll 1
            for (int t = 0; t < 64; ++t) {
                u32x4 a1; u32x2 a2;
                gld128_dev<0>(a1, kvh + t * KVT, l * 16); gld64_dev<0>(a2, kvh + t * KVT, k2rel);
                wait_vm<0>(); PIN(a1); PIN(a2);
                f32x16 s = MFMA8(a2, qz, zero); s = MFMA32(__builtin_bit_cast(bf16x8, a1), qa, s); m = fmaxf(m, hmax16(s));
            }
            m = max_halves(m);
            f32x16 of = {};
#pragma unroll 1
            for (int t = 0; t < 64; ++t) {
                u32x4 a1, b0, b1; u32x2 a2;
                gld128_dev<0>(a1, kvh + t * KVT, l * 16); gld64_dev<0>(a2, kvh + t * KVT, k2rel);
                gld128_dev<0>(b0, kvh + t * KVT, vrel); gld128_dev<0>(b1, kvh + t * KVT, vrel + (r < 20 ? 640 : 0));
                wait_vm<0>(); PIN(a1); PIN(a2); PIN(b0); PIN(b1);
                f32x16 s = MFMA8(a2, qz, zero); s = MFMA32(__builtin_bit_cast(bf16x8, a1), qa, s);
                bf16x8 pa, pb;
#pragma unroll
                for (int e = 0; e < 4; ++e) { CV(pa, e, EX(s[2 * e] - m), EX(s[2 * e + 1] - m)); CV(pb, e, EX(s[8 + 2 * e] - m), EX(s[9 + 2 * e] - m)); }
                of = MFMA32(__builtin_bit_cast(bf16x8, b0), pa, of); of = MFMA32(__builtin_bit_cast(bf16x8, b1), pb, of);
            }
            const f32x4 rf[3] = {{of[0], of[1], of[2], of[3]}, {of[4], of[5], of[6], of[7]}, {of[8], of[9], of[10], of[11]}};
            put(i, rf);
        }
    }
#undef CV
#undef EX
    publish(lds, C_ODONE, 4, ws, F_O + qb * 32 + head);
}

constexpr int CODE_LINES = 208;
#define NOPS(n) do { SBAR(); asm volatile("s_nop %0" :: "i"(n)); SBAR(); } while (0)

__global__ __launch_bounds__(1024, 4) void k_all(Args a) {
    __shared__ __attribute__((aligned(16))) char smem[163840];
    lptr lds = (lptr)smem;
    const char* pc0; asm volatile("s_getpc_b64 %0" : "=s"(pc0));
    char* __restrict__ ws = a.ws;
    const int b = blockIdx.x, xcd = b & 7, bi = b >> 3;
    const int w = __builtin_amdgcn_readfirstlane(threadIdx.x >> 6);
    const int rp = (xcd >> 1) * 4 + (bi < 28 ? (bi & 3) : 2 * (xcd & 1) + (bi & 1)), ct = bi < 28 ? 8 * (xcd & 1) + (bi >> 2) : (bi < 30 ? 7 : 15);
    const int head = xcd + 8 * (bi & 3), qb = bi >> 2;
    if (__builtin_expect(w >= 12, 1)) {
            __builtin_amdgcn_s_setprio(3);
            const int hw = w - 12, l = lane_id();
            const unsigned lds0 = (unsigned)(uintptr_t)lds;
            const char* xa = a.x + (size_t)rp * 128 * 1280;
            const char* wb = a.wqkv + (ct < 15 ? ct : 7) * 256;
            const char* wc = a.w1 + ct * 192;
            unsigned va[4], vb[4], vc[3];
#pragma unroll
            for (int i = 0; i < 4; ++i) {
                va[i] = (unsigned)((32 * hw + 8 * i + (l >> 3)) * 1280 + (((l & 7) ^ (4 * (i & 1) + (l >> 4))) << 4));
                vb[i] = (unsigned)((16 * hw + 4 * i + (l >> 4)) * 3840 + (((l & 15) ^ ((l >> 4) << 2)) << 4));
            }
#pragma unroll
            for (int i = 0; i < 3; ++i) { const int c = 64 * i + l, rho = c / 12; vc[i] = (unsigned)((16 * hw + rho) * 3072 + (c - rho * 12) * 16); }
            auto ldw1 = [&](int k) {
#pragma unroll
                for (int i = 0; i < 3; ++i) glds16<0, false>(wc + (size_t)k * 64 * 3072, vc[i], lds0 + slot_of(W1_TAB, k) + hw * 3072 + i * 1024);
            };
            unsigned wdst = lds0 + WQ_BASE + hw * 4096;
#pragma unroll 1
            for (int kt = 0; kt < 10; ++kt) {
                if (kt >= 2) { if (kt == 6 || kt == 9) wait_vm<8>(); else wait_vm<11>(); __builtin_amdgcn_s_barrier(); }
                const unsigned xdst = lds0 + slot_of(X_TAB, kt) + hw * 4096;
#pragma unroll
                for (int i = 0; i < 4; ++i) { glds16<0, false>(xa, va[i], xdst + i * 1024); glds16<0, false>(wb, vb[i], wdst + i * 1024); }
                if (kt != 5 && kt < 8) ldw1(kt < 5 ? kt : kt - 1);
                xa += 128; wb += 64 * 3840;
                wdst = wdst == lds0 + WQ_BASE + 32768 + hw * 4096 ? wdst - 32768 : wdst + 16384;
            }
#pragma unroll 1
            for (int k = 7; k < 10; ++k) {
                if (k == 7) wait_vm<8>(); else if (k == 8) wait_vm<3>();
                __builtin_amdgcn_s_barrier();
                ldw1(k);
            }
            LBAR();
            if (hw < 2) {
                const LAS f32x2* p = (const LAS f32x2*)(lds + L_PST) + hw * 64 + l;
                const f32x2 t = (p[0] + p[128]) + (p[256] + p[384]);
                const float mean = t.x * (1.0f / 640.0f);
                const float var = fmaxf(opq(t.y) * (1.0f / 640.0f) - mean * mean, 0.f);
                const float rs = __builtin_amdgcn_rsqf(var + 1e-5f), a1 = rs * (ct < 5 ? QSCALE : 1.0f), a2 = rs * __builtin_amdgcn_rsqf(var * rs * rs + 1e-5f);
                *(LAS f32x2*)(lds + L_FST + (hw * 64 + l) * 8) = (f32x2){a1, -a1 * mean};
                *(LAS f32x2*)(lds + L_FS2 + (hw * 64 + l) * 8) = (f32x2){a2, -a2 * mean};
            }
            LBAR();
            wait_vm<6>(); cw_set(lds, C_XFULL + hw, 8);
            wait_vm<3>(); cw_set(lds, C_XFULL + hw, 9);
            wait_vm<0>(); cw_set(lds, C_XFULL + hw, 10);
        }
    if (w < 8) {
        __builtin_amdgcn_s_setprio(A_PRIOQ);
        if (ct < 10) qkv_tile<false>(w, rp, ct, ws, lds);
        else qkv_tile<true>(w, rp, ct, ws, lds);
        int wa = w; asm volatile("" : "+s"(wa));
        attention(wa, head, qb, (ct >= 5 && ct < 15 && (rp & 7) >= LATE_FROM) ? F_QKV + ct * 16 + rp : 0, ws, lds);
        return;
    }
    if (w == 10 && bi < CODE_LINES / 8 && lane_id() < 8) touch(pc0, (bi * 8 + lane_id()) * 128, (unsigned)(uintptr_t)lds + L_DUMMY);
    if (w < 12) { __builtin_amdgcn_s_setprio(M_PRIO); h_tile(w, rp, ct, a.b1, ws, lds); }
    const int rb = 4 * xcd + (bi & 3), nt = bi >> 2;
    const unsigned lds0 = (unsigned)(uintptr_t)lds;
    const int l = lane_id(), g4 = l >> 4, q4 = (l & 15) >> 2, p4 = l & 3, r16 = l & 15;
    const unsigned l16 = l * 16;
    const int w8 = w - 8, strip = w8 & 1, kq = w8 >> 1;
    const unsigned aoff = (unsigned)(g4 * 512 + r16 * 16);
    const unsigned b2off = (unsigned)(W2_BASE + (16 * (g4 >> 1) + 4 * (g4 & 1) + q4) * 160 + p4 * 8);
    f32x4 acc[2][5];
    unsigned vw[5], vw1[5], vw2[5];
    auto w2_offsets = [&](int kind, unsigned (&vw)[5]) {
#pragma unroll
        for (int i = 0; i < 5; ++i) {
            const int c = 64 * i + l, rho = c / 10, ch = c - rho * 10, e = (rho >> 2) & 3;
            const int row = kind == 0 ? rho : (kind == 1 ? rho + (rho >> 4) * 4 : (4 * (rho >> 4) + 2 * (e & 1) + (e >> 1)) * 20 + 16 + (rho & 3));
            vw[i] = (unsigned)(row * 1280 + ch * 16); PIN(vw[i]);
        }
    };
    auto stb = [&](int c) {
        const int j = w8 & 1;
        const unsigned dst = lds0 + (unsigned)(W2_BASE + (c & 3) * W2_STAGE + j * 10240);
        const int gs = c < 12 ? 32 : (c < 16 ? 40 : 160);
        const char* p = (c < 12 ? a.w2 + (size_t)c * 128 * 1280 : a.wo + (size_t)(c < 16 ? (c - 12) * 160 : 0) * 1280) + (size_t)(2 * j * gs) * 1280 + nt * 160;
#pragma unroll
        for (int h = 0; h < 2; ++h)
#pragma unroll
            for (int i = 0; i < 5; ++i) glds16<0, false>(p + (size_t)h * gs * 1280, vw[i], dst + h * 5120 + i * 1024);
    };
    const int h3 = (w8 >> 1) & 1, st16 = 2 * strip + (w8 >> 2);
    const unsigned boff = b2off + (unsigned)(h3 * 10240);
    bf16x8 bq[2][5];
    auto rdq = [&](int j, int i, int n) { lptr sb = lds + (j & 3) * W2_STAGE + i * 5120 + n * 32 + boff; bq[i][n] = cat8(lds_tr(sb), lds_tr(sb + 1280)); };
    auto load_b12 = [&]() {
        cw_wait<2, LSL>(lds, C_WFULL, 13);
#pragma unroll
        for (int n = 0; n < 10; ++n) { rdq(0, n / 5, n % 5); PIN(bq[n / 5][n % 5]); }
        cw_add(lds, C_B12);
    };
    if (w8 < 4) {
        const char* asrc = ws + WS_AF + ((size_t)(2 * rb + strip) * NKS + 4 * kq) * 1024;
        u32x4 an[3][4];
        u32x2 bt[5];
#pragma unroll
        for (int n = 0; n < 5; ++n) {
            u32x2 t = *(const u32x2*)(a.b2 + nt * 160 + n * 32 + g4 * 8); if (kq) t = (u32x2){};
            bt[n] = t;
        }
        const unsigned afull = cw_addr(lds, C_WFULL + (l & 1)), afree = cw_addr(lds, C_WFREE + w8);
        if (w8 == 0) { flag_wait<8>(ws, F_H + (rb >> 1) * 16 + (l & 15), get_tag(lds)); cw_set(lds, C_HRDY, 1); }
        else cw_wait<1, 8>(lds, C_HRDY, 1);
#pragma unroll
        for (int n = 0; n < 5; ++n) { acc[0][n] = bf4_to_f32(bt[n]); acc[1][n] = bf4_to_f32(bt[n]); PIN(acc[0][n]); PIN(acc[1][n]); }
#pragma unroll
        for (int u = 0; u < 3; ++u) {
            const char* p = asrc + u * 8192;
            gld128_dev<0>(an[u][0], p, aoff); gld128_dev<256>(an[u][1], p, aoff); gld128_dev<2048>(an[u][2], p, aoff); gld128_dev<2304>(an[u][3], p, aoff);
        }
#pragma unroll 1
        for (int c3 = 0; c3 < 12; c3 += 3) {
#pragma unroll
            for (int u = 0; u < 3; ++u) {
                const int c = c3 + u;
                cw_wait_at<4>(afull, 1 + c);
                if (c3 < 9 || u == 0) wait_vm<8>(); else if (u == 1) wait_vm<4>(); else wait_vm<0>();
#pragma unroll
                for (int i = 0; i < 4; ++i) PIN(an[u][i]);
                lptr sb = lds + (c & 3) * W2_STAGE + kq * (64 * 160) + b2off;
#pragma unroll
                for (int i = 0; i < 2; ++i) {
                    bf16x8 bw[5];
#pragma unroll
                    for (int n = 0; n < 5; ++n) bw[n] = cat8(lds_tr(sb + i * 5120 + n * 32), lds_tr(sb + i * 5120 + n * 32 + 1280));
                    if (i == 1) cw_set_at<1>(afree, 1 + c);
#pragma unroll
                    for (int n = 0; n < 5; ++n) {
                        acc[0][n] = MFMA16(bw[n], __builtin_bit_cast(bf16x8, an[u][2 * i]), acc[0][n]); PIN(acc[0][n]); NOPS(PACE);
                        acc[1][n] = MFMA16(bw[n], __builtin_bit_cast(bf16x8, an[u][2 * i + 1]), acc[1][n]); PIN(acc[1][n]); NOPS(PACE);
                    }
                }
                SBAR();
                if (c3 < 9) {
                    const char* p = asrc + (c + 3) * 8192;
                    gld128_dev_into<0>(an[u][0], p, aoff); gld128_dev_into<256>(an[u][1], p, aoff); gld128_dev_into<2048>(an[u][2], p, aoff); gld128_dev_into<2304>(an[u][3], p, aoff);
                }
            }
        }
    } else {
        const int hw = w8 - 4;
        __builtin_amdgcn_s_setprio(3);
        const int j = hw & 1;
        const unsigned tag = get_tag(lds);
#pragma unroll
        for (int n = 0; n < 5; ++n) { acc[0][n] = (f32x4){}; PIN(acc[0][n]); }
        if (hw < 2) {
            const int c0 = head * 20, ta = c0 >> 7, tb = (c0 + 19) >> 7;
            const int kvf = F_QKV + (5 + 5 * (l & 1) + ((l & 2) ? tb : ta)) * 16 + 8 * hw;
            const char* src = ws + WS_KV + (size_t)(head * 64 + hw * 32) * KVT;
            const unsigned afull = cw_addr(lds, C_KVFULL + 4 * hw + (l & 3)), afree = cw_addr(lds, C_KVFREE + 4 * hw + (l & 3));
            Polls pl;
            if (hw == 1) { __builtin_amdgcn_s_setprio(WPRIO); cw_wait<1, WSL>(lds, C_KVGO, 1); __builtin_amdgcn_s_setprio(3); }
            else {
                __builtin_amdgcn_s_sleep(POLL0);
                flag_wait_fast(pl, ws, (l & 8) ? F_QKV + ((l & 2) ? tb : ta) * 16 + 2 * qb + (l & 1) : kvf + 2 * (l & 4), tag);
                cw_set(lds, C_KVGO, 1);
            }
            cw_wait<4, 1>(lds, C_XFREE, 7);
            int buf = 0;
#pragma unroll 1
            for (int s = 0; s < 8; ++s) {
                if (s == 3) flag_wait<4>(ws, kvf + ((l >> 2) & 7), tag);
                if (s >= 3) cw_wait_at<LSL>(afree, s - 2);
                const unsigned dst = lds0 + (unsigned)(hw * KV_HALF + buf * KV_STAGE);
                const char* p = src + s * KV_STAGE;
                glds_4k(p, l16, dst); glds_4k(p + 4096, l16, dst + 4096);
                glds16<0, true>(p + 8192, l16, dst + 8192); glds16<1024, true>(p + 8192, l16, dst + 8192);
                if (l < 32) glds16<2048, true>(p + 8192, l16, dst + 8192);
                if (s == 0) {
                    const int f12 = kvf + 1 + ((l >> 2) & 1);
                    unsigned fv = __hip_atomic_load((gu32*)(ws + WS_FLAG) + f12 * 32, __ATOMIC_RELAXED, __HIP_MEMORY_SCOPE_AGENT);
                    wait_vm<1>(); cw_set_at<4>(afull, 1);
                    if (!__all(fv == tag)) flag_wait<2>(ws, f12, tag);
                    cw_wait<4, 1>(lds, C_XFREE, 9);
                } else if (s == 2) { wait_vm<11>(); cw_set_at<4>(afull, 2); }
                if (s >= 2) { wait_vm<0>(); cw_set_at<4>(afull, s < 7 ? s + 1 : 9); }
                buf = buf == 2 ? 0 : buf + 1;
            }
            pl.release();
            if (b == 0 && hw == 0) {
#pragma unroll 1
                for (int i = 0; i < 4; ++i) flag_wait<16>(ws, F_H + 64 * i + l, tag);
                flag_set(ws, 0, tag);
            }
        } else {
            __builtin_amdgcn_s_setprio(2);
            w2_offsets(0, vw); w2_offsets(1, vw1); w2_offsets(2, vw2);
            const unsigned afull = cw_addr(lds, C_WFULL + j), afree = cw_addr(lds, C_WFREE + (l & 3));
            cw_wait<1, LSL>(lds, C_KVFULL + 4 * j, W2_AFTER);
            cw_wait<4, 4>(lds, C_XFREE, 10);
#pragma unroll 1
            for (int c = 0; c < 16; ++c) {
                if (c == 12) {
#pragma unroll
                    for (int i = 0; i < 5; ++i) vw[i] = vw1[i];
                }
                if (c >= 4) cw_wait_at<LSL>(afree, c - 3);
                stb(c);
                if (c >= 3) { wait_vm<10>(); cw_set_at<1>(afull, c); }
            }
            wait_vm<0>(); cw_set_at<1>(afull, 16);
        }
    }
    load_b12();
    if (w8 >= 6) {
        cw_wait<1, 2>(lds, C_B12, 8);
#pragma unroll
        for (int i = 0; i < 5; ++i) vw[i] = vw2[i];
        stb(16);
    }
    cw_wait<4, LSL>(lds, C_KVFREE + 4, 8);
    unsigned eoff[3], loff[3];
    u32x2 xi[3];
#pragma unroll
    for (int j = 0; j < 3; ++j) {
        const int l = lane_id(), u = (64 * j + l) < 160 ? 64 * j + l : l, row = u / 20, qd = u - row * 20;
        eoff[j] = (unsigned)(row * 1280 + qd * 8); loff[j] = (unsigned)(((8 * w8 + row) * 20 + (qd ^ ((row >> 1) & 3))) * 16);
        PIN(eoff[j]); PIN(loff[j]);
        gld64<0>(xi[j], a.x + (size_t)(rb * 64 + 8 * w8) * 1280 + nt * 160, eoff[j]);
    }
    unsigned woff = (unsigned)((r16 * 20 + (g4 ^ ((r16 >> 1) & 3))) * 16);
    Polls pl;
    wait_vm<0>();
    if (w8 < 4) {
        cw_wait<1, 2>(lds, C_ODONE, 4);
#pragma unroll
        for (int n = 0; n < 5; ++n) *(LAS f32x4*)(lds + 40960 + h3 * 10240 + strip * 5120 + woff + n * 64) = acc[1][n];
    } else if (w8 == 4) {
        cw_wait<1, 2>(lds, C_ODONE, 1);
        flag_wait_fast(pl, ws, F_O + xcd * 32 + (l & 31), get_tag(lds));
    }
    const char* osrc = ws + WS_AF + ((size_t)(2 * rb + strip) * NKS + 96 + 4 * h3) * 1024 + (w8 >> 2) * 256;
    char* odst = a.out + (size_t)(rb * 64 + 8 * w8) * 1280 + nt * 160;
    woff += h3 * 20480 + st16 * 5120; PIN(woff);
    asm volatile("" : "+s"(osrc), "+s"(odst));
    LBAR();
    u32x4 on[5][2];
#pragma unroll
    for (int j = 0; j < 5; ++j) { gld128_dev<0>(on[j][0], osrc + j * 8192, aoff); gld128_dev<2048>(on[j][1], osrc + j * 8192, aoff); }
    static_for<0, 10>([&](auto S) {
        constexpr int j = S / 2, i = S % 2;
        wait_vm<9 - S>();
        if (S == 0) pl.release();
        PIN(on[j][i]);
#pragma unroll
        for (int n = 0; n < 5; ++n) {
            acc[0][n] = MFMA16(bq[i][n], __builtin_bit_cast(bf16x8, on[j][i]), acc[0][n]); PIN(acc[0][n]);
            if (j < 4) rdq(j + 1, i, n);
            SBAR();
        }
    });
#pragma unroll
    for (int n = 0; n < 5; ++n) *(LAS f32x4*)(lds + woff + n * 64) = acc[0][n];
    f32x4 xb[3];
#pragma unroll
    for (int j = 0; j < 3; ++j) { PIN(xi[j]); xb[j] = bf4_to_f32(xi[j]); }
    if (w8 & 2) {
#pragma unroll
        for (int j = 0; j < 3; ++j) { lptr p = lds + 40960 - 5120 - (w8 >> 2) * 5120 + loff[j]; xb[j] += *(const LAS f32x4*)p + *(const LAS f32x4*)(p + 10240); }
    }
    LBAR();
#pragma unroll
    for (int j = 0; j < 3; ++j) {
        lptr p = lds + loff[j];
        const f32x4 v = (*(const LAS f32x4*)p + *(const LAS f32x4*)(p + 20480)) + xb[j];
        gst64_out(odst, eoff[j], __builtin_bit_cast(u32x2, __builtin_convertvector(v, bf16x4)));
    }
}

extern "C" void kernel_launch(void* const* d_in, const int* in_sizes, int n_in, void* d_out, int out_size, void* d_ws, size_t ws_size, hipStream_t stream) {
    Args a;
    a.x = (const char*)d_in[0]; a.wqkv = (const char*)d_in[1]; a.wo = (const char*)d_in[2]; a.w1 = (const char*)d_in[4]; a.b1 = (const char*)d_in[5];
    a.w2 = (const char*)d_in[6]; a.b2 = (const char*)d_in[7]; a.ws = (char*)d_ws; a.out = (char*)d_out;
    k_all<<<256, 1024, 0, stream>>>(a);
}
```
